```python
import torch

def _softmax(x, axis=-1):
    return torch.softmax(x, dim=axis)
def _gelu(x):
    return torch.nn.functional.gelu(x, approximate="tanh")
def _silu(x):
    return torch.nn.functional.silu(x)
def _sigmoid(x):
    return torch.sigmoid(x)
def _layernorm(x, axis=-1, eps=1e-5):
    m = x.mean(dim=axis, keepdim=True)
    v = x.var(dim=axis, keepdim=True, unbiased=False)
    return (x - m) / torch.sqrt(v + eps)
def _scatter_add(tgt, idx, src, axis=0):
    return tgt.clone().index_add_(axis, idx, src)
def _segment_sum(src, seg, n):
    out = torch.zeros((n,) + src.shape[1:], dtype=src.dtype)
    return out.index_add_(0, seg, src)
def _pad(x, pw):
    flat = tuple(v for pair in reversed(pw) for v in pair)
    return torch.nn.functional.pad(x, flat)

kernel_name = 'gated_mix__ssm_conv_s4__swiglu_ffn__prompt'

def setup_inputs():

    _w = (lambda a: (lambda t: t.to(torch.bfloat16) if t.is_floating_point() else t)(torch.as_tensor(a)))
    def _A_setup():
        B, L, D, N = 1, 2048, 512, 128
        A_re = -(lambda _x: torch.exp(torch.as_tensor(_x)))((lambda *s: torch.randn(*s, dtype=torch.bfloat16) * 3.2)(D, N))
        A_im = torch.pi * (lambda *s: torch.randn(*s, dtype=torch.bfloat16) * 0.05)(D, N)
        B_re = (lambda *s: torch.randn(*s, dtype=torch.bfloat16) * 0.1)(D, N)
        B_im = (lambda *s: torch.randn(*s, dtype=torch.bfloat16) * 0.1)(D, N)
        C_re = (lambda *s: torch.randn(*s, dtype=torch.bfloat16) * 0.1)(D, N)
        C_im = (lambda *s: torch.randn(*s, dtype=torch.bfloat16) * 0.1)(D, N)
        log_dt = (lambda *s: torch.randn(*s, dtype=torch.bfloat16) * 0.05)(D) * 0.5 - 4.0
        Dskip = (lambda *s: torch.randn(*s, dtype=torch.bfloat16) * 0.05)(D)
        return {
            "u": _w((lambda *s: torch.randn(*s, dtype=torch.bfloat16) * 0.05)(B, L, D)),
            "A_re": _w(A_re),
            "A_im": _w(A_im),
            "B_re": _w(B_re),
            "B_im": _w(B_im),
            "C_re": _w(C_re),
            "C_im": _w(C_im),
            "log_dt": _w(log_dt),
            "Dskip": _w(Dskip),
        }
    _A_in = _A_setup()

    def _B_setup():
        B, S, D, F = 1, 2048, 512, 1792
        return {
            "x": _w((lambda *s: torch.randn(*s, dtype=torch.bfloat16) * 0.05)(B, S, D)),
            "w_gate": _w((lambda *s: torch.randn(*s, dtype=torch.bfloat16) * 0.05)(D, F)),
            "w_up": _w((lambda *s: torch.randn(*s, dtype=torch.bfloat16) * 0.05)(D, F)),
            "w_down": _w((lambda *s: torch.randn(*s, dtype=torch.bfloat16) * 0.05)(F, D)),
        }
    _B_in = _B_setup()

    return {
        "A_u": _A_in["u"],
        "A_A_re": _A_in["A_re"],
        "A_A_im": _A_in["A_im"],
        "A_B_re": _A_in["B_re"],
        "A_B_im": _A_in["B_im"],
        "A_C_re": _A_in["C_re"],
        "A_C_im": _A_in["C_im"],
        "A_log_dt": _A_in["log_dt"],
        "A_Dskip": _A_in["Dskip"],
        "B_x": _B_in["x"],
        "B_w_gate": _B_in["w_gate"],
        "B_w_up": _B_in["w_up"],
        "B_w_down": _B_in["w_down"],
        "gate_w": _w((lambda *s: torch.randn(*s, dtype=torch.bfloat16) * 0.05)(512) * 16.0),
    }

def reference(A_u, A_A_re, A_A_im, A_B_re, A_B_im, A_C_re, A_C_im, A_log_dt, A_Dskip, B_x, B_w_gate, B_w_up, B_w_down, gate_w):
    def _A_ref(u, A_re, A_im, B_re, B_im, C_re, C_im, log_dt, Dskip):
        Bsz, L, D = u.shape
        dt = torch.exp(log_dt)[:, None]
        A = A_re + 1j * A_im
        Bc = B_re + 1j * B_im
        Cc = C_re + 1j * C_im
        logdA = dt * A
        dB = dt * Bc
        l = (lambda x, dt: x.to(dt))(torch.arange(L), A_re.dtype)
        P = torch.exp(logdA[:, :, None] * l[None, None, :])
        K = torch.einsum("dn,dnl,dn->dl", Cc, P, dB).real
        L2 = 2 * L
        Kp = _pad(K, ((0, 0), (0, L)))
        up = (lambda x, *d: x.permute(*d))(u, 0, 2, 1)
        up = _pad(up, ((0, 0), (0, 0), (0, L)))
        Kf = (lambda x, axes=(-2,-1): torch.fft.rfft2(x.float(), dim=axes))(Kp[:, None, :], axes=(-2, -1))
        Uf = (lambda x, axes=(-2,-1): torch.fft.rfft2(x.float(), dim=axes))(up[:, :, None, :], axes=(-2, -1))
        Yf = Uf * Kf[None, :, :, :]
        y = (lambda x, s, axes=(-2,-1): torch.fft.irfft2(x, s=s, dim=axes).to(torch.bfloat16))(Yf, (1, L2), axes=(-2, -1))[:, :, 0, :L]
        y = (lambda x, *d: x.permute(*d))(y, 0, 2, 1) + u * Dskip[None, None, :]
        return y

    def _B_ref(x, w_gate, w_up, w_down):
        g = _silu(x @ w_gate)
        u = x @ w_up
        return (g * u) @ w_down

    x = A_u
    g = _sigmoid((x * gate_w).sum(-1, keepdims=True))
    a = _A_ref(x, A_A_re, A_A_im, A_B_re, A_B_im, A_C_re, A_C_im, A_log_dt, A_Dskip)
    b = _B_ref(x, B_w_gate, B_w_up, B_w_down)
    return g * a + (1 - g) * b
```

```cpp
#include <hip/hip_runtime.h>
#include <hip/hip_bf16.h>
#include <stdint.h>

#if defined(__HIP_DEVICE_COMPILE__)
#pragma clang attribute push(__attribute__((target("no-packed-fp32-ops"))), apply_to = function)
#endif
typedef unsigned short u16;
typedef __attribute__((ext_vector_type(4))) short s16x4;
typedef __attribute__((ext_vector_type(8))) short s16x8;
typedef __attribute__((ext_vector_type(8))) __bf16 bf16x8;
typedef __attribute__((ext_vector_type(2))) __bf16 bf16x2;
typedef __attribute__((ext_vector_type(8))) _Float16 f16x8;
typedef __attribute__((ext_vector_type(2))) _Float16 f16x2;
typedef __attribute__((ext_vector_type(16))) float f32x16;
typedef __attribute__((ext_vector_type(4))) float f32x4;
typedef __attribute__((ext_vector_type(2))) float f32x2;
typedef __attribute__((ext_vector_type(4))) unsigned u32x4;
typedef __attribute__((ext_vector_type(2))) unsigned u32x2;
#define LDS __attribute__((address_space(3)))
#define GLB __attribute__((address_space(1)))

constexpr int L_ = 2048, D_ = 512, N_ = 128, F_ = 1792;
constexpr int KS2 = F_ / 16;
constexpr int KROW = 2080;
constexpr size_t WS_H    = 0;
constexpr size_t WS_WD   = WS_H + (size_t)64 * KS2 * 1024;
constexpr size_t WS_UT   = WS_WD + (size_t)16 * KS2 * 1024;
constexpr size_t WS_KR   = WS_UT + (size_t)D_ * L_ * 2;
constexpr size_t WS_AT   = WS_KR + (size_t)D_ * KROW * 2 + 4096;
constexpr size_t WS_G    = WS_AT + (size_t)D_ * L_ * 2;
constexpr size_t WS_FLAG = WS_G + (size_t)L_ * 4;

__device__ __forceinline__ float u2f(unsigned v) { return __builtin_bit_cast(float, v); }
__device__ __forceinline__ unsigned f2u(float v) { return __builtin_bit_cast(unsigned, v); }
__device__ __forceinline__ void syncthreads() {
  __builtin_amdgcn_fence(__ATOMIC_RELEASE, "workgroup"); __builtin_amdgcn_s_barrier(); __builtin_amdgcn_fence(__ATOMIC_ACQUIRE, "workgroup"); }
__device__ __forceinline__ float shx(float v, int lane, int m) {
  return u2f((unsigned)__builtin_amdgcn_ds_bpermute((lane ^ m) << 2, (int)f2u(v))); }
__device__ __forceinline__ float bf2f(u16 v) { return u2f((unsigned)v << 16); }
__device__ __forceinline__ unsigned pk_bf16(float a, float b) {
  f32x2 t = {a, b}; bf16x2 r = __builtin_convertvector(t, bf16x2); return __builtin_bit_cast(unsigned, r); }
__device__ __forceinline__ float bflo(unsigned p) { return u2f(p << 16); }
__device__ __forceinline__ float bfhi(unsigned p) { return u2f(p & 0xffff0000u); }
__device__ __forceinline__ float bfr(float a) { return bflo(pk_bf16(a, 0.f)); }
__device__ __forceinline__ unsigned pk_f16(float a, float b) {
  f32x2 t = {a, b}; f16x2 r = __builtin_convertvector(t, f16x2); return __builtin_bit_cast(unsigned, r); }
__device__ __forceinline__ float sigmoidf_(float x) { return __builtin_amdgcn_rcpf(1.f + __builtin_amdgcn_exp2f(-1.44269504089f * x)); }
#define WAIT_V(n) asm volatile("s_waitcnt vmcnt(" #n ")" ::: "memory")
#define WAIT_L(n) asm volatile("s_waitcnt lgkmcnt(" #n ")" ::: "memory")
#define BAR() __builtin_amdgcn_s_barrier()
#define SCHED() __builtin_amdgcn_sched_barrier(0)
__device__ __forceinline__ void glds16(const void* g, LDS char* l) {
  __builtin_amdgcn_global_load_lds((const GLB void*)g, (LDS void*)l, 16, 0, 0); }

__device__ __forceinline__ s16x4 trread(LDS char* p) { return __builtin_amdgcn_ds_read_tr16_b64_v4i16((LDS s16x4*)p); }
__device__ __forceinline__ bf16x8 cat8(s16x4 a, s16x4 b) {
  s16x8 r = __builtin_shufflevector(a, b, 0, 1, 2, 3, 4, 5, 6, 7); return __builtin_bit_cast(bf16x8, r); }

struct P1 {
  const u16 *u, *are, *aim, *bre, *bim, *cre, *cim, *logdt, *x, *wg, *wu, *wd, *gw;
  char* ws;
};

template <int S> struct G1Stage;
__device__ __forceinline__ void gemm1(const P1& p, LDS char* sm, int b) {
  const int tid = threadIdx.x, w = __builtin_amdgcn_readfirstlane(tid >> 6), l = tid & 63;
  const int xcd = b & 7, idx = b >> 3;
  const int mt = 4 * (xcd >> 1) + (idx & 3), nt = 7 * (xcd & 1) + (idx >> 2);
  const int wm = w & 1, wn = w >> 1;
  const int krow = 4 * w + (l >> 4);
  const u16* srcg = p.wg + (size_t)krow * F_ + nt * 128 + 8 * ((l & 15) ^ (2 * (l >> 4)));
  const u16* srcu = p.wu + (size_t)krow * F_ + nt * 128 + 8 * ((l & 15) ^ (2 * ((l >> 4) | 4)));
  const int xr0 = 16 * w + (l >> 3);
  const u16* srcx0 = p.x + (size_t)(mt * 128 + xr0) * D_ + 8 * ((l & 7) ^ ((xr0 >> 1) & 7));
  const u16* srcx1 = p.x + (size_t)(mt * 128 + xr0 + 8) * D_ + 8 * ((l & 7) ^ (((xr0 + 8) >> 1) & 7));
  auto issueW = [&](int s) {
    LDS char* d = sm + (s % 3) * 16384 + w * 1024;
    glds16(srcg + (size_t)s * 32 * F_, d);
    glds16(srcu + (size_t)s * 32 * F_, d + 8192); };
  auto issueX = [&](int j) {
    LDS char* d = sm + 49152 + (j & 1) * 16384 + w * 2048;
    glds16(srcx0 + j * 64, d);
    glds16(srcx1 + j * 64, d + 1024); };
  const int h = l >> 5, blk = (l >> 4) & 1, q = (l & 15) >> 2, pp = l & 3, img = pp >> 1;
  const int abase0 = img * 8192 + (8 * h + q) * 256 + 32 * ((2 * wn) ^ (q | (img << 2))) + 16 * blk + 8 * (pp & 1);
  const int abase1 = abase0 ^ 32;
  const int xrow = 64 * wm + (l & 31);
  const int bbase = 49152 + xrow * 128 + 16 * (h ^ ((xrow >> 1) & 7));
  f32x16 acc[2][2];
#pragma unroll
  for (int i = 0; i < 2; ++i)
#pragma unroll
    for (int j = 0; j < 2; ++j)
#pragma unroll
      for (int r = 0; r < 16; ++r) acc[i][j][r] = 0.f;
  bf16x8 fa[2][2], fb[2][2];
  auto loadF = [&](int buf, int s, int ks) {
    const int wo = (s % 3) * 16384 + ks * 16 * 256;
    fa[buf][0] = cat8(trread(sm + abase0 + wo), trread(sm + abase0 + wo + 1024));
    fa[buf][1] = cat8(trread(sm + abase1 + wo), trread(sm + abase1 + wo + 1024));
    const int cc = (4 * (s & 1) + 2 * ks) << 4;
    const int xo = ((s >> 1) & 1) * 16384;
    fb[buf][0] = *(LDS bf16x8*)(sm + ((bbase ^ cc) + xo));
    fb[buf][1] = *(LDS bf16x8*)(sm + ((bbase ^ cc) + xo + 4096)); };
  auto mma = [&](int buf) {
#pragma unroll
    for (int i = 0; i < 2; ++i)
#pragma unroll
      for (int j = 0; j < 2; ++j)
        acc[i][j] = __builtin_amdgcn_mfma_f32_32x32x16_bf16(fa[buf][i], fb[buf][j], acc[i][j], 0, 0, 0); };
  issueX(0); issueW(0); issueW(1);
  WAIT_V(2); BAR();
  loadF(0, 0, 0);
#pragma unroll
  for (int s = 0; s < 16; ++s) {
    if ((s & 1) == 0 && s / 2 + 1 < 8) issueX(s / 2 + 1);
    if (s + 2 < 16) issueW(s + 2);
    loadF(1, s, 1);
    mma(0);
    if (s < 15) {
      WAIT_L(0);
      if (s >= 14) { WAIT_V(0); } else if ((s & 1) == 0) { WAIT_V(4); } else { WAIT_V(2); }
      BAR();
      loadF(0, s + 1, 0);
    }
    mma(1);
  }
  char* H = p.ws + WS_H;
#pragma unroll
  for (int i = 0; i < 2; ++i)
#pragma unroll
    for (int j = 0; j < 2; ++j) {
      unsigned o[4];
#pragma unroll
      for (int hh = 0; hh < 2; ++hh) {
        float hv[4];
#pragma unroll
        for (int e = 0; e < 4; e += 2) {
          unsigned gp = pk_bf16(acc[i][j][8 * hh + e], acc[i][j][8 * hh + e + 1]);
          unsigned up = pk_bf16(acc[i][j][8 * hh + 4 + e], acc[i][j][8 * hh + 4 + e + 1]);
          float g0 = bflo(gp), g1 = bfhi(gp);
          unsigned sp = pk_bf16(g0 * sigmoidf_(g0), g1 * sigmoidf_(g1));
          hv[e] = bflo(sp) * bflo(up); hv[e + 1] = bfhi(sp) * bfhi(up);
        }
        o[2 * hh] = pk_bf16(hv[0], hv[1]); o[2 * hh + 1] = pk_bf16(hv[2], hv[3]);
      }
      const int tb = 4 * mt + 2 * wm + j, ks = 8 * nt + 2 * wn + i;
      u32x4 ov = {o[0], o[1], o[2], o[3]};
      *(u32x4*)(H + ((size_t)(tb * KS2 + ks) * 1024 + l * 16)) = ov;
    }
}

constexpr int PP_P = 0;
constexpr int PP_Q = PP_P + 2 * 41 * 528;
constexpr int PP_KC = PP_Q + 2 * 16 * 528;
constexpr int PP_T = PP_KC + 2 * 656 * 4;
constexpr int PP_W = PP_T + 64 * 132;
constexpr int PP_S = PP_W + 7 * 1024;
static_assert(PP_S + 16 <= 81920, "lds");
static_assert(PP_W % 16 == 0 && PP_KC % 16 == 0 && PP_T % 16 == 0, "al");

__device__ __forceinline__ void cmul(float ar, float ai, float br, float bi, float& r, float& i) {
  r = ar * br - ai * bi; i = ar * bi + ai * br; }

__device__ __forceinline__ void prep(const P1& p, LDS char* sm, int pb) {
  const int tid = threadIdx.x, w = __builtin_amdgcn_readfirstlane(tid >> 6), l = tid & 63;
  if (w < 4) {
    const int ch = tid >> 7, n = tid & 127, d = 2 * pb + ch;
    const int o = d * N_ + n, o2 = d * N_ + (n ^ 64);
    const float are = bf2f(p.are[o]), aim = bf2f(p.aim[o]);
    const float bre = bf2f(p.bre[o]), bim = bf2f(p.bim[o]), cre = bf2f(p.cre[o]), cim = bf2f(p.cim[o]);
    const float bre2 = bf2f(p.bre[o2]), bim2 = bf2f(p.bim[o2]), cre2 = bf2f(p.cre[o2]), cim2 = bf2f(p.cim[o2]);
    const float dt = bfr(expf(bf2f(p.logdt[d])));
    const float zr = dt * are, zi = dt * aim;
    float wr, wi, wr2, wi2;
    cmul(cre, cim, dt * bre, dt * bim, wr, wi);
    cmul(cre2, cim2, dt * bre2, dt * bim2, wr2, wi2);
    float m = fmaxf(fmaxf(fabsf(wr), fabsf(wi)), fmaxf(fabsf(wr2), fabsf(wi2)));
#pragma unroll
    for (int s = 32; s >= 1; s >>= 1) m = fmaxf(m, shx(m, l, s));
    int ex = (int)((f2u(m) >> 23) & 255);
    ex = ex < 2 ? 2 : (ex > 250 ? 250 : ex);
    const float sc = u2f((unsigned)(253 - ex) << 23), isc = u2f((unsigned)(ex + 1) << 23);
    wr *= sc; wi *= sc;
    if (n == 0) *(LDS float*)(sm + PP_S + 4 * ch) = isc;
    const float zl = zr * 1.44269504089f, rv = zi * 0.15915494309f;
    auto E = [&](float x, float& er, float& ei) {
      float mag = __builtin_amdgcn_exp2f(zl * x), ph = __builtin_amdgcn_fractf(rv * x);
      er = mag * __builtin_amdgcn_cosf(ph); ei = mag * __builtin_amdgcn_sinf(ph); };
    LDS char* Pb = sm + PP_P + ch * (41 * 528) + n * 4;
    LDS char* Qb = sm + PP_Q + ch * (16 * 528) + n * 4;
    auto stP = [&](int row, float r, float i) { *(LDS unsigned*)(Pb + row * 528) = pk_f16(r, i); };
    auto stQ = [&](int row, float r, float i) { *(LDS unsigned*)(Qb + row * 528) = pk_f16(r, -i); };
    {
      float qr[16], qi[16];
      qr[0] = 1.f; qi[0] = 0.f; E(8.f, qr[1], qi[1]);
#pragma unroll
      for (int j = 2; j < 16; ++j) cmul(qr[j / 2], qi[j / 2], qr[j - j / 2], qi[j - j / 2], qr[j], qi[j]);
#pragma unroll
      for (int j = 0; j < 16; ++j) stQ(j, qr[j], qi[j]);
    }
    float e128r, e128i, e256r, e256i, e512r, e512i, e1kr, e1ki, e2kr, e2ki;
    E(128.f, e128r, e128i); E(256.f, e256r, e256i); E(512.f, e512r, e512i); E(1024.f, e1kr, e1ki); E(2048.f, e2kr, e2ki);
    float ar[16], ai[16];
    {
      float er[8], ei[8];
      er[0] = 1.f; ei[0] = 0.f; E(1.f, er[1], ei[1]);
#pragma unroll
      for (int j = 2; j < 8; ++j) cmul(er[j / 2], ei[j / 2], er[j - j / 2], ei[j - j / 2], er[j], ei[j]);
      ar[0] = wr; ai[0] = wi;
#pragma unroll
      for (int j = 1; j < 8; ++j) cmul(wr, wi, er[j], ei[j], ar[j], ai[j]);
#pragma unroll
      for (int j = 0; j < 8; ++j) cmul(ar[j], ai[j], e128r, e128i, ar[8 + j], ai[8 + j]);
#pragma unroll
      for (int j = 0; j < 16; ++j) stP(j, ar[j], ai[j]);
    }
    float br[8], bi[8];
#pragma unroll
    for (int j = 0; j < 8; ++j) { cmul(ar[2 * (j & 3) + 8 * (j >> 2)], ai[2 * (j & 3) + 8 * (j >> 2)], e256r, e256i, br[j], bi[j]); stP(16 + j, br[j], bi[j]); }
    float cr[8], ci[8];
#pragma unroll
    for (int j = 0; j < 8; ++j) {
      const int e = j & 1, ip = j >> 1;
      float sr = ip < 2 ? ar[4 * e + 8 * ip] : br[2 * e + 4 * (ip - 2)], si = ip < 2 ? ai[4 * e + 8 * ip] : bi[2 * e + 4 * (ip - 2)];
      cmul(sr, si, e512r, e512i, cr[j], ci[j]); stP(24 + j, cr[j], ci[j]); }
#pragma unroll
    for (int j = 0; j < 8; ++j) {
      float sr = j < 2 ? ar[8 * j] : (j < 4 ? br[4 * (j - 2)] : cr[2 * (j - 4)]);
      float si = j < 2 ? ai[8 * j] : (j < 4 ? bi[4 * (j - 2)] : ci[2 * (j - 4)]);
      float r, i; cmul(sr, si, e1kr, e1ki, r, i); stP(32 + j, r, i); }
    { float r, i; cmul(wr, wi, e2kr, e2ki, r, i); stP(40, r, i); }
    syncthreads();
    for (int job = w; job < 6; job += 4) {
      const int c2 = job >= 3, T = job - 3 * c2;
      LDS char* pa = sm + PP_P + c2 * (41 * 528) + (16 * T + (l & 15)) * 528 + (l >> 4) * 16;
      LDS char* pq = sm + PP_Q + c2 * (16 * 528) + (l & 15) * 528 + (l >> 4) * 16;
      f32x4 acc = {0.f, 0.f, 0.f, 0.f};
#pragma unroll
      for (int s = 0; s < 8; ++s)
        acc = __builtin_amdgcn_mfma_f32_16x16x32_f16(*(LDS f16x8*)(pa + s * 64), *(LDS f16x8*)(pq + s * 64), acc, 0, 0, 0);
      const float is = *(LDS float*)(sm + PP_S + 4 * c2);
      LDS float* kc = (LDS float*)(sm + PP_KC) + c2 * 656;
      const int j = l & 15;
#pragma unroll
      for (int r = 0; r < 4; ++r) {
        const int row = 4 * (l >> 4) + r;
        int ci_;
        if (T == 0) ci_ = (row & 7) + 128 * (row >> 3) + 8 * j;
        else if (T == 1) ci_ = row < 8 ? 256 + (row & 3) + 64 * (row >> 2) + 4 * j : 384 + (row & 1) + 32 * ((row - 8) >> 1) + 2 * j;
        else ci_ = row < 8 ? 512 + 16 * row + j : ((row == 8 && j == 0) ? 640 : 641);
        kc[ci_] = acc[r] * is;
      }
    }
    syncthreads();
    u16* kr = (u16*)(p.ws + WS_KR);
    for (int job = tid; job < 520; job += 256) {
      const int c2 = job >= 260, c = job - 260 * c2;
      LDS float* kc = (LDS float*)(sm + PP_KC) + c2 * 656;
      float v[8];
#pragma unroll
      for (int e = 0; e < 8; ++e) {
        const int mm = 2047 - (8 * c + e);
        const unsigned bits = f2u((float)(mm < 0 ? 0 : mm));
        const unsigned r = (bits + 0x7FFFu + ((bits >> 16) & 1u)) >> 16;
        const int ci_ = mm < 128 ? (mm < 0 ? 0 : mm) : (int)r - 0x4300 + 128;
        const float kv = kc[ci_];
        v[e] = mm < 0 ? 0.f : kv;
      }
      u32x4 ov = {pk_bf16(v[0], v[1]), pk_bf16(v[2], v[3]), pk_bf16(v[4], v[5]), pk_bf16(v[6], v[7])};
      *(u32x4*)(kr + (size_t)(2 * pb + c2) * KROW + 8 * c) = ov;
    }
  } else {
    const int t = tid - 256, wv = w - 4;
    for (int i = wv; i < 7; i += 4) {
      const int f = 7 * pb + i, s = f >> 4, db = f & 15;
      glds16(p.wd + (size_t)(16 * s + (l >> 2)) * D_ + 32 * db + 8 * (l & 3), sm + PP_W + i * 1024);
    }
    const int lt = pb >> 3, dtile = pb & 7;
    u32x4 t0 = *(const u32x4*)(p.u + (size_t)(64 * lt + (t >> 3)) * D_ + 64 * dtile + 8 * (t & 7));
    u32x4 t1 = *(const u32x4*)(p.u + (size_t)(64 * lt + 32 + (t >> 3)) * D_ + 64 * dtile + 8 * (t & 7));
    u32x4 gwv = *(const u32x4*)(p.gw + 8 * l);
    float gs[2];
#pragma unroll
    for (int k = 0; k < 2; ++k) {
      u32x4 xv = *(const u32x4*)(p.u + (size_t)(8 * pb + 2 * wv + k) * D_ + 8 * l);
      float s = 0.f;
#pragma unroll
      for (int e = 0; e < 4; ++e) {
        unsigned pr = pk_bf16(bflo(xv[e]) * bflo(gwv[e]), bfhi(xv[e]) * bfhi(gwv[e]));
        s += bflo(pr); s += bfhi(pr);
      }
#pragma unroll
      for (int o = 32; o >= 1; o >>= 1) s += shx(s, l, o);
      gs[k] = s;
    }
    if (l == 0) {
      float* g = (float*)(p.ws + WS_G);
      g[8 * pb + 2 * wv] = bfr(sigmoidf_(bfr(gs[0])));
      g[8 * pb + 2 * wv + 1] = bfr(sigmoidf_(bfr(gs[1])));
    }
    if (pb == 0 && t < 8) *(unsigned*)(p.ws + WS_FLAG + 256 * t) = 0u;
#pragma unroll
    for (int e = 0; e < 4; ++e) {
      *(LDS unsigned*)(sm + PP_T + (t >> 3) * 132 + (t & 7) * 16 + 4 * e) = t0[e];
      *(LDS unsigned*)(sm + PP_T + (32 + (t >> 3)) * 132 + (t & 7) * 16 + 4 * e) = t1[e];
    }
    WAIT_V(0);
    {
      const int h = l >> 5, blk = (l >> 4) & 1, q = (l & 15) >> 2, pp = l & 3;
      for (int i = wv; i < 7; i += 4) {
        const int f = 7 * pb + i, s = f >> 4, db = f & 15;
        LDS char* b0 = sm + PP_W + i * 1024 + (4 * h + q) * 64 + (16 * blk + 4 * pp) * 2;
        s16x4 a = trread(b0), b = trread(b0 + 8 * 64);
        s16x8 r = __builtin_shufflevector(a, b, 0, 1, 2, 3, 4, 5, 6, 7);
        *(s16x8*)(p.ws + WS_WD + ((size_t)(db * KS2 + s) * 1024 + l * 16)) = r;
      }
    }
    syncthreads();
    {
      const int dd = t >> 2, lc = (t & 3) * 16;
      unsigned o[8];
#pragma unroll
      for (int e = 0; e < 8; ++e) {
        u16 a = *(LDS u16*)(sm + PP_T + (lc + 2 * e) * 132 + 2 * dd), b = *(LDS u16*)(sm + PP_T + (lc + 2 * e + 1) * 132 + 2 * dd);
        o[e] = (unsigned)a | ((unsigned)b << 16);
      }
      u16* ut = (u16*)(p.ws + WS_UT) + (size_t)(64 * dtile + dd) * L_ + 64 * lt + lc;
      u32x4 o0 = {o[0], o[1], o[2], o[3]}, o1 = {o[4], o[5], o[6], o[7]};
      *(u32x4*)ut = o0; *(u32x4*)(ut + 8) = o1;
    }
    syncthreads();
  }
}

__global__ void __launch_bounds__(512, 4) k1(P1 p) {
  extern __shared__ __attribute__((aligned(16))) char smem[];
  LDS char* sm = (LDS char*)smem;
    if (blockIdx.x < 224) gemm1(p, sm, blockIdx.x); else prep(p, sm, blockIdx.x - 224);
}

struct P2 { const u16* dskip; u16* out; char* ws; };
constexpr int CV_K = 0;
constexpr int CV_U = 2 * 4 * 4160;
constexpr int CV_R = CV_U + 2 * 4608;
constexpr int K2_LDS = 65536 + 8192 + 64;

__device__ __forceinline__ void conv(const P2& p, LDS char* sm, int cb) {
  const int tid = threadIdx.x, w = __builtin_amdgcn_readfirstlane(tid >> 6), l = tid & 63;
  const u16* ut = (const u16*)(p.ws + WS_UT) + (size_t)(2 * cb) * L_;
  const u16* kr = (const u16*)(p.ws + WS_KR) + (size_t)(2 * cb) * KROW;
  u32x4 uv[2];
#pragma unroll
  for (int c = 0; c < 2; ++c) uv[c] = *(const u32x4*)(ut + c * L_ + 8 * tid);
  u32x4 k0[3]; u32x2 k1v[3];
#pragma unroll
  for (int i = 0; i < 3; ++i) {
    int job = tid + 256 * i; job = job < 520 ? job : 519;
    const int c2 = job >= 260, c = job - 260 * c2;
    k0[i] = *(const u32x4*)(kr + c2 * KROW + 8 * c);
    k1v[i] = *(const u32x2*)(kr + c2 * KROW + 8 * c + 8);
    if (c == 259) { k1v[i][0] = 0u; k1v[i][1] = 0u; }
  }
  const float dsk = bf2f(p.dskip[2 * cb + (w >> 1)]);
#pragma unroll
  for (int c = 0; c < 2; ++c) {
    *(LDS u32x4*)(sm + CV_U + c * 4608 + 512 + 16 * tid) = uv[c];
    if (tid < 32) { u32x4 z = {0u, 0u, 0u, 0u}; *(LDS u32x4*)(sm + CV_U + c * 4608 + 16 * tid) = z; }
  }
#pragma unroll
  for (int i = 0; i < 3; ++i) {
    const int job = tid + 256 * i;
    if (job < 520) {
      const int c2 = job >= 260, c = job - 260 * c2;
      unsigned dd[6] = {k0[i][0], k0[i][1], k0[i][2], k0[i][3], k1v[i][0], k1v[i][1]};
      LDS char* b = sm + CV_K + c2 * (4 * 4160) + 16 * c;
      u32x4 c0 = {dd[0], dd[1], dd[2], dd[3]}, c2v = {dd[1], dd[2], dd[3], dd[4]}, c1, c3;
#pragma unroll
      for (int e = 0; e < 4; ++e) { c1[e] = __builtin_amdgcn_alignbit(dd[e + 1], dd[e], 16); c3[e] = __builtin_amdgcn_alignbit(dd[e + 2], dd[e + 1], 16); }
      *(LDS u32x4*)(b) = c0; *(LDS u32x4*)(b + 4160) = c1; *(LDS u32x4*)(b + 2 * 4160) = c2v; *(LDS u32x4*)(b + 3 * 4160) = c3;
    }
  }
  syncthreads();
  const int ch = w >> 1, rh = w & 1, j = l & 15, g = l >> 4;
  const int e = 3 - (j & 3);
  LDS char* ka = sm + CV_K + ch * (4 * 4160) + e * 4160 + 2 * (2031 - j + 8 * g - e);
  LDS char* ub = sm + CV_U + ch * 4608 + 2 * (240 + 16 * j + 8 * g);
  f32x4 acc[8];
#pragma unroll
  for (int c = 0; c < 8; ++c) acc[c] = f32x4{0.f, 0.f, 0.f, 0.f};
#pragma unroll
  for (int rr = 0; rr < 4; ++rr) {
    const int r = 4 * rh + rr;
    bf16x8 bf[8];
#pragma unroll
    for (int t = 0; t < 8; ++t) bf[t] = *(LDS bf16x8*)(ub + 512 * t - 64 * r);
#pragma unroll
    for (int q = 0; q < 8; ++q) {
      LDS char* a = ka - 64 * (8 * q + r);
      s16x4 lo = *(LDS s16x4*)a, hi = *(LDS s16x4*)(a + 8);
      bf16x8 af = cat8(lo, hi);
#pragma unroll
      for (int c = q; c < 8; ++c) acc[c] = __builtin_amdgcn_mfma_f32_16x16x32_bf16(af, bf[c - q], acc[c], 0, 0, 0);
    }
  }
  LDS char* rb = sm + CV_R;
#pragma unroll
  for (int c = 0; c < 4; ++c) *(LDS f32x4*)(rb + (w * 4 + c) * 1024 + l * 16) = acc[4 * (rh ^ 1) + c];
  syncthreads();
  u16* at = (u16*)(p.ws + WS_AT) + (size_t)(2 * cb + ch) * L_;
#pragma unroll
  for (int c = 0; c < 4; ++c) {
    f32x4 o = *(LDS f32x4*)(rb + ((w ^ 1) * 4 + c) * 1024 + l * 16);
    f32x4 y = acc[4 * rh + c] + o;
    const int lpos = 256 * (4 * rh + c) + 16 * j + 4 * g;
    u32x2 uu = *(LDS u32x2*)(sm + CV_U + ch * 4608 + 512 + 2 * lpos);
    unsigned y0 = pk_bf16(y[0], y[1]), y1 = pk_bf16(y[2], y[3]);
    unsigned s0 = pk_bf16(bflo(uu[0]) * dsk, bfhi(uu[0]) * dsk), s1 = pk_bf16(bflo(uu[1]) * dsk, bfhi(uu[1]) * dsk);
    unsigned a0 = pk_bf16(bflo(y0) + bflo(s0), bfhi(y0) + bfhi(s0)), a1 = pk_bf16(bflo(y1) + bflo(s1), bfhi(y1) + bfhi(s1));
    unsigned long long v = (unsigned long long)a0 | ((unsigned long long)a1 << 32);
    __hip_atomic_store((GLB unsigned long long*)(at + lpos), v, __ATOMIC_RELAXED, __HIP_MEMORY_SCOPE_AGENT);
  }
  WAIT_V(0);
  syncthreads();
  if (tid == 0) __hip_atomic_fetch_add((GLB unsigned*)(p.ws + WS_FLAG + 256 * (cb >> 5)), 1u, __ATOMIC_RELAXED, __HIP_MEMORY_SCOPE_AGENT);
}

constexpr int PF = 8;
__device__ __forceinline__ void gemm2(const P2& p, LDS char* sm, int b) {
  const int tid = threadIdx.x, w = __builtin_amdgcn_readfirstlane(tid >> 6), l = tid & 63;
  const int xcd = b & 7, idx = b >> 3;
  const int mt = 4 * xcd + (idx & 3), nt = idx >> 2;
  const char* A0 = p.ws + WS_WD + ((size_t)(2 * nt) * KS2 + 28 * w) * 1024 + l * 16;
  const char* B0 = p.ws + WS_H + ((size_t)(2 * mt) * KS2 + 28 * w) * 1024 + l * 16;
  f32x16 acc[2][2];
#pragma unroll
  for (int i = 0; i < 2; ++i)
#pragma unroll
    for (int j = 0; j < 2; ++j)
#pragma unroll
      for (int r = 0; r < 16; ++r) acc[i][j][r] = 0.f;
  bf16x8 fa[PF][2], fb[PF][2];
  auto ld = [&](int s) {
    fa[s % PF][0] = *(const bf16x8*)(A0 + s * 1024); fb[s % PF][0] = *(const bf16x8*)(B0 + s * 1024);
    fa[s % PF][1] = *(const bf16x8*)(A0 + (KS2 + s) * 1024); fb[s % PF][1] = *(const bf16x8*)(B0 + (KS2 + s) * 1024); };
#pragma unroll
  for (int s = 0; s < PF; ++s) ld(s);
#pragma unroll
  for (int s = 0; s < 28; ++s) {
    bf16x8 a0 = fa[s % PF][0], a1 = fa[s % PF][1], b0 = fb[s % PF][0], b1 = fb[s % PF][1];
    acc[0][0] = __builtin_amdgcn_mfma_f32_32x32x16_bf16(a0, b0, acc[0][0], 0, 0, 0);
    acc[0][1] = __builtin_amdgcn_mfma_f32_32x32x16_bf16(a0, b1, acc[0][1], 0, 0, 0);
    acc[1][0] = __builtin_amdgcn_mfma_f32_32x32x16_bf16(a1, b0, acc[1][0], 0, 0, 0);
    acc[1][1] = __builtin_amdgcn_mfma_f32_32x32x16_bf16(a1, b1, acc[1][1], 0, 0, 0);
    SCHED();
    if (s + PF < 28) ld(s + PF);
    SCHED();
  }
#pragma unroll
  for (int i = 0; i < 2; ++i)
#pragma unroll
    for (int j = 0; j < 2; ++j)
#pragma unroll
      for (int q = 0; q < 4; ++q) {
        f32x4 v = {acc[i][j][4 * q], acc[i][j][4 * q + 1], acc[i][j][4 * q + 2], acc[i][j][4 * q + 3]};
        *(LDS f32x4*)(sm + w * 16384 + ((2 * i + j) * 4 + q) * 1024 + l * 16) = v;
      }
  if (tid == 0) {
    GLB unsigned* flag = (GLB unsigned*)(p.ws + WS_FLAG + 256 * nt);
    unsigned spins = 0;
    while (__hip_atomic_load(flag, __ATOMIC_RELAXED, __HIP_MEMORY_SCOPE_AGENT) < 32u) {
      __builtin_amdgcn_s_sleep(2);
      if (++spins > (1u << 24)) break;
    }
  }
  syncthreads();
  {
    const u16* at = (const u16*)(p.ws + WS_AT) + (size_t)(64 * nt) * L_ + 64 * mt;
#pragma unroll
    for (int i = 0; i < 4; ++i) {
      const int c = tid + 256 * i, row = c >> 4, ch = c & 15;
      unsigned long long v = __hip_atomic_load((GLB unsigned long long*)(at + (size_t)row * L_ + 4 * ch), __ATOMIC_RELAXED, __HIP_MEMORY_SCOPE_AGENT);
      *(LDS unsigned long long*)(sm + 65536 + row * 128 + ch * 8) = v;
    }
  }
  const int bi = w >> 1, cb = w & 1;
  f32x4 bsum[4];
#pragma unroll
  for (int q = 0; q < 4; ++q) {
    f32x4 s = *(LDS f32x4*)(sm + (w * 4 + q) * 1024 + l * 16);
#pragma unroll
    for (int ww = 1; ww < 4; ++ww) s += *(LDS f32x4*)(sm + ww * 16384 + (w * 4 + q) * 1024 + l * 16);
    bsum[q] = s;
  }
  const int tok = 64 * mt + 32 * cb + (l & 31), h = l >> 5;
  const float g = ((const float*)(p.ws + WS_G))[tok];
  const float og = bfr(1.f - g);
  syncthreads();
#pragma unroll
  for (int q = 0; q < 4; ++q) {
    const int qq = (l & 15) >> 2, pp = l & 3, blk = (l >> 4) & 1;
    s16x4 av = trread(sm + 65536 + (32 * bi + 8 * q + 4 * h + qq) * 128 + (32 * cb + 16 * blk + 4 * pp) * 2);
    float o[4];
#pragma unroll
    for (int e = 0; e < 4; ++e) {
      float a = bf2f((u16)av[e]);
      o[e] = bfr(g * a) + bfr(og * bfr(bsum[q][e]));
    }
    u32x2 ov = {pk_bf16(o[0], o[1]), pk_bf16(o[2], o[3])};
    *(u32x2*)(p.out + (size_t)tok * D_ + 64 * nt + 32 * bi + 8 * q + 4 * h) = ov;
  }
}

__global__ void __launch_bounds__(256, 2) k2(P2 p) {
  extern __shared__ __attribute__((aligned(16))) char smem[];
  LDS char* sm = (LDS char*)smem;
  if (blockIdx.x < 256) conv(p, sm, blockIdx.x); else gemm2(p, sm, blockIdx.x - 256);
}

#if defined(__HIP_DEVICE_COMPILE__)
#pragma clang attribute pop
#endif
extern "C" void kernel_launch(void* const* d_in, const int* in_sizes, int n_in, void* d_out, int out_size,
                              void* d_ws, size_t ws_size, hipStream_t stream) {
  P1 a;
  a.u = (const u16*)d_in[0]; a.are = (const u16*)d_in[1]; a.aim = (const u16*)d_in[2]; a.bre = (const u16*)d_in[3];
  a.bim = (const u16*)d_in[4]; a.cre = (const u16*)d_in[5]; a.cim = (const u16*)d_in[6]; a.logdt = (const u16*)d_in[7];
  a.x = (const u16*)d_in[0]; a.wg = (const u16*)d_in[10]; a.wu = (const u16*)d_in[11]; a.wd = (const u16*)d_in[12];
  a.gw = (const u16*)d_in[13]; a.ws = (char*)d_ws;
  P2 b; b.dskip = (const u16*)d_in[8]; b.out = (u16*)d_out; b.ws = (char*)d_ws;
  hipFuncSetAttribute((const void*)k1, hipFuncAttributeMaxDynamicSharedMemorySize, 81920);
  hipFuncSetAttribute((const void*)k2, hipFuncAttributeMaxDynamicSharedMemorySize, K2_LDS);
  hipLaunchKernelGGL(k1, dim3(480), dim3(512), 81920, stream, a);
  hipLaunchKernelGGL(k2, dim3(512), dim3(256), K2_LDS, stream, b);
}
```

```cpp
#include <hip/hip_runtime.h>
#include <hip/hip_bf16.h>
#include <stdint.h>

#if defined(__HIP_DEVICE_COMPILE__)
#pragma clang attribute push(__attribute__((target("no-packed-fp32-ops"))), apply_to = function)
#endif
typedef unsigned short u16;
typedef __attribute__((ext_vector_type(4))) short s16x4;
typedef __attribute__((ext_vector_type(8))) short s16x8;
typedef __attribute__((ext_vector_type(8))) __bf16 bf16x8;
typedef __attribute__((ext_vector_type(2))) __bf16 bf16x2;
typedef __attribute__((ext_vector_type(8))) _Float16 f16x8;
typedef __attribute__((ext_vector_type(2))) _Float16 f16x2;
typedef __attribute__((ext_vector_type(16))) float f32x16;
typedef __attribute__((ext_vector_type(4))) float f32x4;
typedef __attribute__((ext_vector_type(2))) float f32x2;
typedef __attribute__((ext_vector_type(4))) unsigned u32x4;
typedef __attribute__((ext_vector_type(2))) unsigned u32x2;
#define LDS __attribute__((address_space(3)))
#define GLB __attribute__((address_space(1)))

constexpr int L_ = 2048, D_ = 512, N_ = 128, F_ = 1792;
constexpr int KS2 = F_ / 16;
constexpr int KROW = 2080;
constexpr size_t WS_H    = 0;
constexpr size_t WS_WD   = WS_H + (size_t)64 * KS2 * 1024;
constexpr size_t WS_UT   = WS_WD + (size_t)16 * KS2 * 1024;
constexpr size_t WS_KR   = WS_UT + (size_t)D_ * L_ * 2;
constexpr size_t WS_AT   = WS_KR + (size_t)D_ * KROW * 2 + 4096;
constexpr size_t WS_G    = WS_AT + (size_t)D_ * L_ * 2;
constexpr size_t WS_FLAG = WS_G + (size_t)L_ * 4;

__device__ __forceinline__ float u2f(unsigned v) { return __builtin_bit_cast(float, v); }
__device__ __forceinline__ unsigned f2u(float v) { return __builtin_bit_cast(unsigned, v); }
__device__ __forceinline__ void syncthreads() {
  __builtin_amdgcn_fence(__ATOMIC_RELEASE, "workgroup"); __builtin_amdgcn_s_barrier(); __builtin_amdgcn_fence(__ATOMIC_ACQUIRE, "workgroup"); }
__device__ __forceinline__ float shx(float v, int lane, int m) {
  return u2f((unsigned)__builtin_amdgcn_ds_bpermute((lane ^ m) << 2, (int)f2u(v))); }
__device__ __forceinline__ float bf2f(u16 v) { return u2f((unsigned)v << 16); }
__device__ __forceinline__ unsigned pk_bf16(float a, float b) {
  f32x2 t = {a, b}; bf16x2 r = __builtin_convertvector(t, bf16x2); return __builtin_bit_cast(unsigned, r); }
__device__ __forceinline__ float bflo(unsigned p) { return u2f(p << 16); }
__device__ __forceinline__ float bfhi(unsigned p) { return u2f(p & 0xffff0000u); }
__device__ __forceinline__ float bfr(float a) { return bflo(pk_bf16(a, 0.f)); }
__device__ __forceinline__ unsigned pk_f16(float a, float b) {
  f32x2 t = {a, b}; f16x2 r = __builtin_convertvector(t, f16x2); return __builtin_bit_cast(unsigned, r); }
__device__ __forceinline__ float sigmoidf_(float x) { return __builtin_amdgcn_rcpf(1.f + __builtin_amdgcn_exp2f(-1.44269504089f * x)); }
#define WAIT_V(n) asm volatile("s_waitcnt vmcnt(" #n ")" ::: "memory")
#define WAIT_L(n) asm volatile("s_waitcnt lgkmcnt(" #n ")" ::: "memory")
#define BAR() __builtin_amdgcn_s_barrier()
#define SCHED() __builtin_amdgcn_sched_barrier(0)
#define ST(ws, row, i)
__device__ __forceinline__ void glds16(const void* g, LDS char* l) {
  __builtin_amdgcn_global_load_lds((const GLB void*)g, (LDS void*)l, 16, 0, 0); }

__device__ __forceinline__ s16x4 trread(LDS char* p) { return __builtin_amdgcn_ds_read_tr16_b64_v4i16((LDS s16x4*)p); }
__device__ __forceinline__ bf16x8 cat8(s16x4 a, s16x4 b) {
  s16x8 r = __builtin_shufflevector(a, b, 0, 1, 2, 3, 4, 5, 6, 7); return __builtin_bit_cast(bf16x8, r); }

struct P1 {
  const u16 *u, *are, *aim, *bre, *bim, *cre, *cim, *logdt, *x, *wg, *wu, *wd, *gw;
  char* ws;
};

#define DSR_TR(dst, addr, off) asm volatile("ds_read_b64_tr_b16 %0, %1 offset:%2" : "=v"(dst) : "v"(addr), "i"(off))
#define DSR_128(dst, addr, off) asm volatile("ds_read_b128 %0, %1 offset:%2" : "=v"(dst) : "v"(addr), "i"(off))
__device__ __forceinline__ void dma16(unsigned voff, const char* sbase, unsigned ldsb) {
  asm volatile("s_mov_b32 m0, %2\n\ts_nop 0\n\tglobal_load_lds_dwordx4 %0, %1" :: "v"(voff), "s"(sbase), "s"(ldsb) : "memory"); }
__device__ __forceinline__ void dma4(unsigned voff, const char* sbase, unsigned ldsb) {
  asm volatile("s_mov_b32 m0, %2\n\ts_nop 0\n\tglobal_load_lds_dword %0, %1" :: "v"(voff), "s"(sbase), "s"(ldsb) : "memory"); }
struct Frag { s16x4 a0l, a0h, a1l, a1h; bf16x8 b0, b1; };
#define FRAG_WAIT(f) asm volatile("s_waitcnt lgkmcnt(0)" : "+v"(f.a0l), "+v"(f.a0h), "+v"(f.a1l), "+v"(f.a1h), "+v"(f.b0), "+v"(f.b1) :: "memory")
#define MFMA32(a, b, c) __builtin_amdgcn_mfma_f32_32x32x16_bf16(a, b, c, 0, 0, 0)
struct G1 {
  unsigned a0, a1, bx[4];
  unsigned vg, vu, vx0, vx1;
  const char *wg, *wu, *x;
  unsigned lw, lx;
};
template <int S, int KS> __device__ __forceinline__ void g1_rd0(const G1& g, Frag& f) {
  constexpr int wo = (S % 3) * 16384 + KS * 4096, xo = ((S >> 1) & 1) * 16384;
  DSR_TR(f.a0l, g.a0, wo); DSR_TR(f.a0h, g.a0, wo + 1024); DSR_128(f.b0, g.bx[2 * (S & 1) + KS], xo); }
template <int S, int KS> __device__ __forceinline__ void g1_rd1(const G1& g, Frag& f) {
  constexpr int wo = (S % 3) * 16384 + KS * 4096, xo = ((S >> 1) & 1) * 16384;
  DSR_128(f.b1, g.bx[2 * (S & 1) + KS], xo + 4096); DSR_TR(f.a1l, g.a1, wo); DSR_TR(f.a1h, g.a1, wo + 1024); }
template <int S> __device__ __forceinline__ void g1_issueW(const G1& g) {
  dma16(g.vg, g.wg + (size_t)S * 32 * F_ * 2, g.lw + (S % 3) * 16384);
  dma16(g.vu, g.wu + (size_t)S * 32 * F_ * 2, g.lw + (S % 3) * 16384 + 8192); }
template <int J> __device__ __forceinline__ void g1_issueX(const G1& g) {
  dma16(g.vx0, g.x + J * 128, g.lx + (J & 1) * 16384);
  dma16(g.vx1, g.x + J * 128, g.lx + (J & 1) * 16384 + 1024); }
template <int S> __device__ __forceinline__ void g1_stage(const G1& g, Frag& f0, Frag& f1, f32x16 (&acc)[2][2]) {
  FRAG_WAIT(f0);
  {
    const bf16x8 a0 = cat8(f0.a0l, f0.a0h), a1 = cat8(f0.a1l, f0.a1h);
    acc[0][0] = MFMA32(a0, f0.b0, acc[0][0]); SCHED();
    g1_rd0<S, 1>(g, f1); SCHED();
    acc[0][1] = MFMA32(a0, f0.b1, acc[0][1]); SCHED();
    g1_rd1<S, 1>(g, f1); SCHED();
    acc[1][0] = MFMA32(a1, f0.b0, acc[1][0]); SCHED();
    if constexpr (S + 2 < 16) g1_issueW<S + 2>(g);
    SCHED();
    acc[1][1] = MFMA32(a1, f0.b1, acc[1][1]); SCHED();
    if constexpr ((S & 1) == 0 && S / 2 + 1 < 8) g1_issueX<S / 2 + 1>(g);
    SCHED();
  }
  FRAG_WAIT(f1);
  if constexpr (S < 15) {
    if constexpr (S >= 14) { WAIT_V(0); } else if constexpr ((S & 1) == 0) { WAIT_V(4); } else { WAIT_V(2); }
    BAR();
  }
  {
    const bf16x8 a0 = cat8(f1.a0l, f1.a0h), a1 = cat8(f1.a1l, f1.a1h);
    acc[0][0] = MFMA32(a0, f1.b0, acc[0][0]); SCHED();
    if constexpr (S < 15) g1_rd0<S + 1, 0>(g, f0);
    SCHED();
    acc[0][1] = MFMA32(a0, f1.b1, acc[0][1]); SCHED();
    if constexpr (S < 15) g1_rd1<S + 1, 0>(g, f0);
    SCHED();
    acc[1][0] = MFMA32(a1, f1.b0, acc[1][0]); SCHED();
    acc[1][1] = MFMA32(a1, f1.b1, acc[1][1]); SCHED();
  }
  if constexpr (S < 15) g1_stage<S + 1>(g, f0, f1, acc);
}
__device__ __forceinline__ void gemm1(const P1& p, LDS char* sm, int b) {
  const int tid = threadIdx.x, w = __builtin_amdgcn_readfirstlane(tid >> 6), l = tid & 63;
  const int xcd = b & 7, idx = b >> 3;
  const int mt = 4 * (xcd >> 1) + (idx & 3), nt = 7 * (xcd & 1) + (idx >> 2);
  const int wm = w & 1, wn = w >> 1;
  const unsigned smb = (unsigned)(size_t)sm;
  G1 g;
  {
    const int krow = 4 * w + (l >> 4);
    g.vg = 2u * (krow * F_ + 8 * ((l & 15) ^ (2 * (l >> 4))));
    g.vu = 2u * (krow * F_ + 8 * ((l & 15) ^ (2 * ((l >> 4) | 4))));
    const int xr0 = 16 * w + (l >> 3);
    g.vx0 = 2u * (xr0 * D_ + 8 * ((l & 7) ^ ((xr0 >> 1) & 7)));
    g.vx1 = 2u * ((xr0 + 8) * D_ + 8 * ((l & 7) ^ (((xr0 + 8) >> 1) & 7)));
    g.wg = (const char*)(p.wg + nt * 128); g.wu = (const char*)(p.wu + nt * 128); g.x = (const char*)(p.x + (size_t)mt * 128 * D_);
    g.lw = smb + w * 1024; g.lx = smb + 49152 + w * 2048;
    const int h = l >> 5, blk = (l >> 4) & 1, q = (l & 15) >> 2, pp = l & 3, img = pp >> 1;
    g.a0 = smb + img * 8192 + (8 * h + q) * 256 + 32 * ((2 * wn) ^ (q | (img << 2))) + 16 * blk + 8 * (pp & 1);
    g.a1 = g.a0 ^ 32;
    const int xrow = 64 * wm + (l & 31);
    const unsigned bb = 49152 + xrow * 128 + 16 * (h ^ ((xrow >> 1) & 7));
#pragma unroll
    for (int c = 0; c < 4; ++c) g.bx[c] = smb + (bb ^ (c * 32));
  }
  f32x16 acc[2][2];
#pragma unroll
  for (int i = 0; i < 2; ++i)
#pragma unroll
    for (int j = 0; j < 2; ++j)
#pragma unroll
      for (int r = 0; r < 16; ++r) acc[i][j][r] = 0.f;
  Frag f0, f1;
  ST(p.ws, b, 0);
  g1_issueX<0>(g); g1_issueW<0>(g); g1_issueW<1>(g);
  WAIT_V(2); BAR();
  ST(p.ws, b, 1);
  g1_rd0<0, 0>(g, f0); g1_rd1<0, 0>(g, f0);
  g1_stage<0>(g, f0, f1, acc);
  ST(p.ws, b, 2);
  char* H = p.ws + WS_H;
#pragma unroll
  for (int i = 0; i < 2; ++i)
#pragma unroll
    for (int j = 0; j < 2; ++j) {
      unsigned o[4];
#pragma unroll
      for (int hh = 0; hh < 2; ++hh) {
        float hv[4];
#pragma unroll
        for (int e = 0; e < 4; e += 2) {
          unsigned gp = pk_bf16(acc[i][j][8 * hh + e], acc[i][j][8 * hh + e + 1]);
          unsigned up = pk_bf16(acc[i][j][8 * hh + 4 + e], acc[i][j][8 * hh + 4 + e + 1]);
          float g0 = bflo(gp), g1 = bfhi(gp);
          unsigned sp = pk_bf16(g0 * sigmoidf_(g0), g1 * sigmoidf_(g1));
          hv[e] = bflo(sp) * bflo(up); hv[e + 1] = bfhi(sp) * bfhi(up);
        }
        o[2 * hh] = pk_bf16(hv[0], hv[1]); o[2 * hh + 1] = pk_bf16(hv[2], hv[3]);
      }
      const int tb = 4 * mt + 2 * wm + j, ks = 8 * nt + 2 * wn + i;
      u32x4 ov = {o[0], o[1], o[2], o[3]};
      *(u32x4*)(H + ((size_t)(tb * KS2 + ks) * 1024 + l * 16)) = ov;
    }
  ST(p.ws, b, 3);
}

constexpr int PP_P = 0;
constexpr int PP_Q = PP_P + 2 * 41 * 528;
constexpr int PP_KC = PP_Q + 2 * 16 * 528;
constexpr int PP_T = PP_KC + 2 * 656 * 4;
constexpr int PP_W = PP_T + 64 * 132;
constexpr int PP_S = PP_W + 7 * 1024;
static_assert(PP_S + 64 + 256 <= 81920, "lds");
static_assert(PP_W % 16 == 0 && PP_KC % 16 == 0 && PP_T % 16 == 0, "al");

__device__ __forceinline__ void cmul(float ar, float ai, float br, float bi, float& r, float& i) {
  r = ar * br - ai * bi; i = ar * bi + ai * br; }

__device__ __forceinline__ void prep(const P1& p, LDS char* sm, int pb) {
  const int tid = threadIdx.x, w = __builtin_amdgcn_readfirstlane(tid >> 6), l = tid & 63;
  ST(p.ws, 224 + pb, 0);
  if (w < 4) {
    const int ch = tid >> 7, n = tid & 127, d = 2 * pb + ch;
    const int o = d * N_ + n, o2 = d * N_ + (n ^ 64);
    const float are = bf2f(p.are[o]), aim = bf2f(p.aim[o]);
    const float bre = bf2f(p.bre[o]), bim = bf2f(p.bim[o]), cre = bf2f(p.cre[o]), cim = bf2f(p.cim[o]);
    const float bre2 = bf2f(p.bre[o2]), bim2 = bf2f(p.bim[o2]), cre2 = bf2f(p.cre[o2]), cim2 = bf2f(p.cim[o2]);
    const float dt = bfr(expf(bf2f(p.logdt[d])));
    const float zr = dt * are, zi = dt * aim;
    float wr, wi, wr2, wi2;
    cmul(cre, cim, dt * bre, dt * bim, wr, wi);
    cmul(cre2, cim2, dt * bre2, dt * bim2, wr2, wi2);
    float m = fmaxf(fmaxf(fabsf(wr), fabsf(wi)), fmaxf(fabsf(wr2), fabsf(wi2)));
#pragma unroll
    for (int s = 32; s >= 1; s >>= 1) m = fmaxf(m, shx(m, l, s));
    int ex = (int)((f2u(m) >> 23) & 255);
    ex = ex < 2 ? 2 : (ex > 250 ? 250 : ex);
    const float sc = u2f((unsigned)(253 - ex) << 23), isc = u2f((unsigned)(ex + 1) << 23);
    wr *= sc; wi *= sc;
    if (n == 0) *(LDS float*)(sm + PP_S + 4 * ch) = isc;
    const float zl = zr * 1.44269504089f, rv = zi * 0.15915494309f;
    auto E = [&](float x, float& er, float& ei) {
      float mag = __builtin_amdgcn_exp2f(zl * x), ph = __builtin_amdgcn_fractf(rv * x);
      er = mag * __builtin_amdgcn_cosf(ph); ei = mag * __builtin_amdgcn_sinf(ph); };
    LDS char* Pb = sm + PP_P + ch * (41 * 528) + n * 4;
    LDS char* Qb = sm + PP_Q + ch * (16 * 528) + n * 4;
    auto stP = [&](int row, float r, float i) { *(LDS unsigned*)(Pb + row * 528) = pk_f16(r, i); };
    auto stQ = [&](int row, float r, float i) { *(LDS unsigned*)(Qb + row * 528) = pk_f16(r, -i); };
    {
      float qr[16], qi[16];
      qr[0] = 1.f; qi[0] = 0.f; E(8.f, qr[1], qi[1]);
#pragma unroll
      for (int j = 2; j < 16; ++j) cmul(qr[j / 2], qi[j / 2], qr[j - j / 2], qi[j - j / 2], qr[j], qi[j]);
#pragma unroll
      for (int j = 0; j < 16; ++j) stQ(j, qr[j], qi[j]);
    }
    float e128r, e128i, e256r, e256i, e512r, e512i, e1kr, e1ki, e2kr, e2ki;
    E(128.f, e128r, e128i); E(256.f, e256r, e256i); E(512.f, e512r, e512i); E(1024.f, e1kr, e1ki); E(2048.f, e2kr, e2ki);
    float ar[16], ai[16];
    {
      float er[8], ei[8];
      er[0] = 1.f; ei[0] = 0.f; E(1.f, er[1], ei[1]);
#pragma unroll
      for (int j = 2; j < 8; ++j) cmul(er[j / 2], ei[j / 2], er[j - j / 2], ei[j - j / 2], er[j], ei[j]);
      ar[0] = wr; ai[0] = wi;
#pragma unroll
      for (int j = 1; j < 8; ++j) cmul(wr, wi, er[j], ei[j], ar[j], ai[j]);
#pragma unroll
      for (int j = 0; j < 8; ++j) cmul(ar[j], ai[j], e128r, e128i, ar[8 + j], ai[8 + j]);
#pragma unroll
      for (int j = 0; j < 16; ++j) stP(j, ar[j], ai[j]);
    }
    float br[8], bi[8];
#pragma unroll
    for (int j = 0; j < 8; ++j) { cmul(ar[2 * (j & 3) + 8 * (j >> 2)], ai[2 * (j & 3) + 8 * (j >> 2)], e256r, e256i, br[j], bi[j]); stP(16 + j, br[j], bi[j]); }
    float cr[8], ci[8];
#pragma unroll
    for (int j = 0; j < 8; ++j) {
      const int e = j & 1, ip = j >> 1;
      float sr = ip < 2 ? ar[4 * e + 8 * ip] : br[2 * e + 4 * (ip - 2)], si = ip < 2 ? ai[4 * e + 8 * ip] : bi[2 * e + 4 * (ip - 2)];
      cmul(sr, si, e512r, e512i, cr[j], ci[j]); stP(24 + j, cr[j], ci[j]); }
#pragma unroll
    for (int j = 0; j < 8; ++j) {
      float sr = j < 2 ? ar[8 * j] : (j < 4 ? br[4 * (j - 2)] : cr[2 * (j - 4)]);
      float si = j < 2 ? ai[8 * j] : (j < 4 ? bi[4 * (j - 2)] : ci[2 * (j - 4)]);
      float r, i; cmul(sr, si, e1kr, e1ki, r, i); stP(32 + j, r, i); }
    { float r, i; cmul(wr, wi, e2kr, e2ki, r, i); stP(40, r, i); }
    ST(p.ws, 224 + pb, 1);
    syncthreads();
    for (int job = w; job < 6; job += 4) {
      const int c2 = job >= 3, T = job - 3 * c2;
      LDS char* pa = sm + PP_P + c2 * (41 * 528) + (16 * T + (l & 15)) * 528 + (l >> 4) * 16;
      LDS char* pq = sm + PP_Q + c2 * (16 * 528) + (l & 15) * 528 + (l >> 4) * 16;
      f32x4 acc = {0.f, 0.f, 0.f, 0.f};
#pragma unroll
      for (int s = 0; s < 8; ++s)
        acc = __builtin_amdgcn_mfma_f32_16x16x32_f16(*(LDS f16x8*)(pa + s * 64), *(LDS f16x8*)(pq + s * 64), acc, 0, 0, 0);
      const float is = *(LDS float*)(sm + PP_S + 4 * c2);
      LDS float* kc = (LDS float*)(sm + PP_KC) + c2 * 656;
      const int j = l & 15;
#pragma unroll
      for (int r = 0; r < 4; ++r) {
        const int row = 4 * (l >> 4) + r;
        int ci_;
        if (T == 0) ci_ = (row & 7) + 128 * (row >> 3) + 8 * j;
        else if (T == 1) ci_ = row < 8 ? 256 + (row & 3) + 64 * (row >> 2) + 4 * j : 384 + (row & 1) + 32 * ((row - 8) >> 1) + 2 * j;
        else ci_ = row < 8 ? 512 + 16 * row + j : ((row == 8 && j == 0) ? 640 : 641);
        kc[ci_] = acc[r] * is;
      }
    }
    syncthreads();
    ST(p.ws, 224 + pb, 2);
    u16* kr = (u16*)(p.ws + WS_KR);
    for (int job = tid; job < 520; job += 256) {
      const int c2 = job >= 260, c = job - 260 * c2;
      LDS float* kc = (LDS float*)(sm + PP_KC) + c2 * 656;
      float v[8];
#pragma unroll
      for (int e = 0; e < 8; ++e) {
        const int mm = 2047 - (8 * c + e);
        const unsigned bits = f2u((float)(mm < 0 ? 0 : mm));
        const unsigned r = (bits + 0x7FFFu + ((bits >> 16) & 1u)) >> 16;
        const int ci_ = mm < 128 ? (mm < 0 ? 0 : mm) : (int)r - 0x4300 + 128;
        const float kv = kc[ci_];
        v[e] = mm < 0 ? 0.f : kv;
      }
      u32x4 ov = {pk_bf16(v[0], v[1]), pk_bf16(v[2], v[3]), pk_bf16(v[4], v[5]), pk_bf16(v[6], v[7])};
      *(u32x4*)(kr + (size_t)(2 * pb + c2) * KROW + 8 * c) = ov;
    }
    ST(p.ws, 224 + pb, 3);
  } else {
    const int t = tid - 256, wv = w - 4;
    {
      const int xcd = pb & 7, i = pb >> 3;
      const unsigned scr = (unsigned)(size_t)sm + PP_S + 64;
#pragma unroll
      for (int k = 0; k < 4; ++k) {
        const int v = t + 256 * k, rm = v / 28, pc = v - 28 * rm;
        const unsigned off = 2u * ((16 * i + (rm >> 1)) * F_ + 896 * (xcd & 1)) + pc * 64;
        if (v < 896) { if (rm & 1) dma4(off, (const char*)p.wu, scr); else dma4(off, (const char*)p.wg, scr); }
      }
      dma4(2u * ((512 * (xcd >> 1) + 256 * (i & 1) + t) * D_) + (i >> 1) * 64, (const char*)p.x, scr);
    }
    for (int i = wv; i < 7; i += 4) {
      const int f = 7 * pb + i, s = f >> 4, db = f & 15;
      glds16(p.wd + (size_t)(16 * s + (l >> 2)) * D_ + 32 * db + 8 * (l & 3), sm + PP_W + i * 1024);
    }
    const int lt = pb >> 3, dtile = pb & 7;
    u32x4 t0 = *(const u32x4*)(p.u + (size_t)(64 * lt + (t >> 3)) * D_ + 64 * dtile + 8 * (t & 7));
    u32x4 t1 = *(const u32x4*)(p.u + (size_t)(64 * lt + 32 + (t >> 3)) * D_ + 64 * dtile + 8 * (t & 7));
    u32x4 gwv = *(const u32x4*)(p.gw + 8 * l);
    float gs[2];
#pragma unroll
    for (int k = 0; k < 2; ++k) {
      u32x4 xv = *(const u32x4*)(p.u + (size_t)(8 * pb + 2 * wv + k) * D_ + 8 * l);
      float s = 0.f;
#pragma unroll
      for (int e = 0; e < 4; ++e) {
        unsigned pr = pk_bf16(bflo(xv[e]) * bflo(gwv[e]), bfhi(xv[e]) * bfhi(gwv[e]));
        s += bflo(pr); s += bfhi(pr);
      }
#pragma unroll
      for (int o = 32; o >= 1; o >>= 1) s += shx(s, l, o);
      gs[k] = s;
    }
    if (l == 0) {
      float* g = (float*)(p.ws + WS_G);
      g[8 * pb + 2 * wv] = bfr(sigmoidf_(bfr(gs[0])));
      g[8 * pb + 2 * wv + 1] = bfr(sigmoidf_(bfr(gs[1])));
    }
    if (pb == 0 && t < 8) *(unsigned*)(p.ws + WS_FLAG + 256 * t) = 0u;
#pragma unroll
    for (int e = 0; e < 4; ++e) {
      *(LDS unsigned*)(sm + PP_T + (t >> 3) * 132 + (t & 7) * 16 + 4 * e) = t0[e];
      *(LDS unsigned*)(sm + PP_T + (32 + (t >> 3)) * 132 + (t & 7) * 16 + 4 * e) = t1[e];
    }
    WAIT_V(0);
    ST(p.ws, 224 + pb, 1);
    {
      const int h = l >> 5, blk = (l >> 4) & 1, q = (l & 15) >> 2, pp = l & 3;
      for (int i = wv; i < 7; i += 4) {
        const int f = 7 * pb + i, s = f >> 4, db = f & 15;
        LDS char* b0 = sm + PP_W + i * 1024 + (4 * h + q) * 64 + (16 * blk + 4 * pp) * 2;
        s16x4 a = trread(b0), b = trread(b0 + 8 * 64);
        s16x8 r = __builtin_shufflevector(a, b, 0, 1, 2, 3, 4, 5, 6, 7);
        *(s16x8*)(p.ws + WS_WD + ((size_t)(db * KS2 + s) * 1024 + l * 16)) = r;
      }
    }
    syncthreads();
    {
      const int dd = t >> 2, lc = (t & 3) * 16;
      unsigned o[8];
#pragma unroll
      for (int e = 0; e < 8; ++e) {
        u16 a = *(LDS u16*)(sm + PP_T + (lc + 2 * e) * 132 + 2 * dd), b = *(LDS u16*)(sm + PP_T + (lc + 2 * e + 1) * 132 + 2 * dd);
        o[e] = (unsigned)a | ((unsigned)b << 16);
      }
      u16* ut = (u16*)(p.ws + WS_UT) + (size_t)(64 * dtile + dd) * L_ + 64 * lt + lc;
      u32x4 o0 = {o[0], o[1], o[2], o[3]}, o1 = {o[4], o[5], o[6], o[7]};
      *(u32x4*)ut = o0; *(u32x4*)(ut + 8) = o1;
    }
    ST(p.ws, 224 + pb, 3);
    syncthreads();
  }
}

__global__ void __launch_bounds__(512, 4) k1(P1 p) {
  extern __shared__ __attribute__((aligned(16))) char smem[];
  LDS char* sm = (LDS char*)smem;
  if (blockIdx.x < 224) gemm1(p, sm, blockIdx.x); else prep(p, sm, blockIdx.x - 224);
}

struct P2 { const u16* dskip; u16* out; char* ws; };
constexpr int CV_K = 0;
constexpr int CV_U = 2 * 4 * 4160;
constexpr int CV_R = CV_U + 2 * 4608;
constexpr int K2_LDS = 65536 + 8192 + 64;

__device__ __forceinline__ void conv(const P2& p, LDS char* sm, int cb) {
  const int tid = threadIdx.x, w = __builtin_amdgcn_readfirstlane(tid >> 6), l = tid & 63;
  ST(p.ws, 512 + cb, 0);
  const u16* ut = (const u16*)(p.ws + WS_UT) + (size_t)(2 * cb) * L_;
  const u16* kr = (const u16*)(p.ws + WS_KR) + (size_t)(2 * cb) * KROW;
  u32x4 uv[2];
#pragma unroll
  for (int c = 0; c < 2; ++c) uv[c] = *(const u32x4*)(ut + c * L_ + 8 * tid);
  u32x4 k0[3]; u32x2 k1v[3];
#pragma unroll
  for (int i = 0; i < 3; ++i) {
    int job = tid + 256 * i; job = job < 520 ? job : 519;
    const int c2 = job >= 260, c = job - 260 * c2;
    k0[i] = *(const u32x4*)(kr + c2 * KROW + 8 * c);
    k1v[i] = *(const u32x2*)(kr + c2 * KROW + 8 * c + 8);
    if (c == 259) { k1v[i][0] = 0u; k1v[i][1] = 0u; }
  }
  const float dsk = bf2f(p.dskip[2 * cb + (w >> 1)]);
#pragma unroll
  for (int c = 0; c < 2; ++c) {
    *(LDS u32x4*)(sm + CV_U + c * 4608 + 512 + 16 * tid) = uv[c];
    if (tid < 32) { u32x4 z = {0u, 0u, 0u, 0u}; *(LDS u32x4*)(sm + CV_U + c * 4608 + 16 * tid) = z; }
  }
#pragma unroll
  for (int i = 0; i < 3; ++i) {
    const int job = tid + 256 * i;
    if (job < 520) {
      const int c2 = job >= 260, c = job - 260 * c2;
      unsigned dd[6] = {k0[i][0], k0[i][1], k0[i][2], k0[i][3], k1v[i][0], k1v[i][1]};
      LDS char* b = sm + CV_K + c2 * (4 * 4160) + 16 * c;
      u32x4 c0 = {dd[0], dd[1], dd[2], dd[3]}, c2v = {dd[1], dd[2], dd[3], dd[4]}, c1, c3;
#pragma unroll
      for (int e = 0; e < 4; ++e) { c1[e] = __builtin_amdgcn_alignbit(dd[e + 1], dd[e], 16); c3[e] = __builtin_amdgcn_alignbit(dd[e + 2], dd[e + 1], 16); }
      *(LDS u32x4*)(b) = c0; *(LDS u32x4*)(b + 4160) = c1; *(LDS u32x4*)(b + 2 * 4160) = c2v; *(LDS u32x4*)(b + 3 * 4160) = c3;
    }
  }
  syncthreads();
  ST(p.ws, 512 + cb, 1);
  const int ch = w >> 1, rh = w & 1, j = l & 15, g = l >> 4;
  const int e = 3 - (j & 3);
  LDS char* ka = sm + CV_K + ch * (4 * 4160) + e * 4160 + 2 * (2031 - j + 8 * g - e);
  LDS char* ub = sm + CV_U + ch * 4608 + 2 * (240 + 16 * j + 8 * g);
  f32x4 acc[8];
#pragma unroll
  for (int c = 0; c < 8; ++c) acc[c] = f32x4{0.f, 0.f, 0.f, 0.f};
#pragma unroll
  for (int rr = 0; rr < 4; ++rr) {
    const int r = 4 * rh + rr;
    bf16x8 bf[8];
#pragma unroll
    for (int t = 0; t < 8; ++t) bf[t] = *(LDS bf16x8*)(ub + 512 * t - 64 * r);
#pragma unroll
    for (int q = 0; q < 8; ++q) {
      LDS char* a = ka - 64 * (8 * q + r);
      s16x4 lo = *(LDS s16x4*)a, hi = *(LDS s16x4*)(a + 8);
      bf16x8 af = cat8(lo, hi);
#pragma unroll
      for (int c = q; c < 8; ++c) acc[c] = __builtin_amdgcn_mfma_f32_16x16x32_bf16(af, bf[c - q], acc[c], 0, 0, 0);
    }
  }
  ST(p.ws, 512 + cb, 2);
  LDS char* rb = sm + CV_R;
#pragma unroll
  for (int c = 0; c < 4; ++c) *(LDS f32x4*)(rb + (w * 4 + c) * 1024 + l * 16) = acc[4 * (rh ^ 1) + c];
  syncthreads();
  u16* at = (u16*)(p.ws + WS_AT) + (size_t)(2 * cb + ch) * L_;
#pragma unroll
  for (int c = 0; c < 4; ++c) {
    f32x4 o = *(LDS f32x4*)(rb + ((w ^ 1) * 4 + c) * 1024 + l * 16);
    f32x4 y = acc[4 * rh + c] + o;
    const int lpos = 256 * (4 * rh + c) + 16 * j + 4 * g;
    u32x2 uu = *(LDS u32x2*)(sm + CV_U + ch * 4608 + 512 + 2 * lpos);
    unsigned y0 = pk_bf16(y[0], y[1]), y1 = pk_bf16(y[2], y[3]);
    unsigned s0 = pk_bf16(bflo(uu[0]) * dsk, bfhi(uu[0]) * dsk), s1 = pk_bf16(bflo(uu[1]) * dsk, bfhi(uu[1]) * dsk);
    unsigned a0 = pk_bf16(bflo(y0) + bflo(s0), bfhi(y0) + bfhi(s0)), a1 = pk_bf16(bflo(y1) + bflo(s1), bfhi(y1) + bfhi(s1));
    unsigned long long v = (unsigned long long)a0 | ((unsigned long long)a1 << 32);
    __hip_atomic_store((GLB unsigned long long*)(at + lpos), v, __ATOMIC_RELAXED, __HIP_MEMORY_SCOPE_AGENT);
  }
  WAIT_V(0);
  syncthreads();
  ST(p.ws, 512 + cb, 3);
  if (tid == 0) __hip_atomic_fetch_add((GLB unsigned*)(p.ws + WS_FLAG + 256 * (cb >> 5)), 1u, __ATOMIC_RELAXED, __HIP_MEMORY_SCOPE_AGENT);
}

constexpr int PF = 8;
__device__ __forceinline__ void gemm2(const P2& p, LDS char* sm, int b) {
  const int tid = threadIdx.x, w = __builtin_amdgcn_readfirstlane(tid >> 6), l = tid & 63;
  const int xcd = b & 7, idx = b >> 3;
  const int mt = 4 * xcd + (idx & 3), nt = idx >> 2;
  const char* A0 = p.ws + WS_WD + ((size_t)(2 * nt) * KS2 + 28 * w) * 1024 + l * 16;
  const char* B0 = p.ws + WS_H + ((size_t)(2 * mt) * KS2 + 28 * w) * 1024 + l * 16;
  f32x16 acc[2][2];
#pragma unroll
  for (int i = 0; i < 2; ++i)
#pragma unroll
    for (int j = 0; j < 2; ++j)
#pragma unroll
      for (int r = 0; r < 16; ++r) acc[i][j][r] = 0.f;
  ST(p.ws, 768 + b, 0);
  bf16x8 fa[PF][2], fb[PF][2];
  auto ld = [&](int s) {
    fa[s % PF][0] = *(const bf16x8*)(A0 + s * 1024); fb[s % PF][0] = *(const bf16x8*)(B0 + s * 1024);
    fa[s % PF][1] = *(const bf16x8*)(A0 + (KS2 + s) * 1024); fb[s % PF][1] = *(const bf16x8*)(B0 + (KS2 + s) * 1024); };
#pragma unroll
  for (int s = 0; s < PF; ++s) ld(s);
#pragma unroll
  for (int s = 0; s < 28; ++s) {
    bf16x8 a0 = fa[s % PF][0], a1 = fa[s % PF][1], b0 = fb[s % PF][0], b1 = fb[s % PF][1];
    acc[0][0] = __builtin_amdgcn_mfma_f32_32x32x16_bf16(a0, b0, acc[0][0], 0, 0, 0);
    acc[0][1] = __builtin_amdgcn_mfma_f32_32x32x16_bf16(a0, b1, acc[0][1], 0, 0, 0);
    acc[1][0] = __builtin_amdgcn_mfma_f32_32x32x16_bf16(a1, b0, acc[1][0], 0, 0, 0);
    acc[1][1] = __builtin_amdgcn_mfma_f32_32x32x16_bf16(a1, b1, acc[1][1], 0, 0, 0);
    SCHED();
    if (s + PF < 28) ld(s + PF);
    SCHED();
  }
  ST(p.ws, 768 + b, 1);
#pragma unroll
  for (int i = 0; i < 2; ++i)
#pragma unroll
    for (int j = 0; j < 2; ++j)
#pragma unroll
      for (int q = 0; q < 4; ++q) {
        f32x4 v = {acc[i][j][4 * q], acc[i][j][4 * q + 1], acc[i][j][4 * q + 2], acc[i][j][4 * q + 3]};
        *(LDS f32x4*)(sm + w * 16384 + ((2 * i + j) * 4 + q) * 1024 + l * 16) = v;
      }
  if (tid == 0) {
    GLB unsigned* flag = (GLB unsigned*)(p.ws + WS_FLAG + 256 * nt);
    unsigned spins = 0;
    while (__hip_atomic_load(flag, __ATOMIC_RELAXED, __HIP_MEMORY_SCOPE_AGENT) < 32u) {
      __builtin_amdgcn_s_sleep(2);
      if (++spins > (1u << 24)) break;
    }
  }
  syncthreads();
  ST(p.ws, 768 + b, 2);
  {
    const u16* at = (const u16*)(p.ws + WS_AT) + (size_t)(64 * nt) * L_ + 64 * mt;
#pragma unroll
    for (int i = 0; i < 4; ++i) {
      const int c = tid + 256 * i, row = c >> 4, ch = c & 15;
      unsigned long long v = __hip_atomic_load((GLB unsigned long long*)(at + (size_t)row * L_ + 4 * ch), __ATOMIC_RELAXED, __HIP_MEMORY_SCOPE_AGENT);
      *(LDS unsigned long long*)(sm + 65536 + row * 128 + ch * 8) = v;
    }
  }
  const int bi = w >> 1, cb = w & 1;
  f32x4 bsum[4];
#pragma unroll
  for (int q = 0; q < 4; ++q) {
    f32x4 s = *(LDS f32x4*)(sm + (w * 4 + q) * 1024 + l * 16);
#pragma unroll
    for (int ww = 1; ww < 4; ++ww) s += *(LDS f32x4*)(sm + ww * 16384 + (w * 4 + q) * 1024 + l * 16);
    bsum[q] = s;
  }
  const int tok = 64 * mt + 32 * cb + (l & 31), h = l >> 5;
  const float g = ((const float*)(p.ws + WS_G))[tok];
  const float og = bfr(1.f - g);
  syncthreads();
#pragma unroll
  for (int q = 0; q < 4; ++q) {
    const int qq = (l & 15) >> 2, pp = l & 3, blk = (l >> 4) & 1;
    s16x4 av = trread(sm + 65536 + (32 * bi + 8 * q + 4 * h + qq) * 128 + (32 * cb + 16 * blk + 4 * pp) * 2);
    float o[4];
#pragma unroll
    for (int e = 0; e < 4; ++e) {
      float a = bf2f((u16)av[e]);
      o[e] = bfr(g * a) + bfr(og * bfr(bsum[q][e]));
    }
    u32x2 ov = {pk_bf16(o[0], o[1]), pk_bf16(o[2], o[3])};
    *(u32x2*)(p.out + (size_t)tok * D_ + 64 * nt + 32 * bi + 8 * q + 4 * h) = ov;
  }
  ST(p.ws, 768 + b, 3);
}

__global__ void __launch_bounds__(256, 2) k2(P2 p) {
  extern __shared__ __attribute__((aligned(16))) char smem[];
  LDS char* sm = (LDS char*)smem;
  if (blockIdx.x < 256) conv(p, sm, blockIdx.x); else gemm2(p, sm, blockIdx.x - 256);
}

#if defined(__HIP_DEVICE_COMPILE__)
#pragma clang attribute pop
#endif
extern "C" void kernel_launch(void* const* d_in, const int* in_sizes, int n_in, void* d_out, int out_size,
                              void* d_ws, size_t ws_size, hipStream_t stream) {
  P1 a;
  a.u = (const u16*)d_in[0]; a.are = (const u16*)d_in[1]; a.aim = (const u16*)d_in[2]; a.bre = (const u16*)d_in[3];
  a.bim = (const u16*)d_in[4]; a.cre = (const u16*)d_in[5]; a.cim = (const u16*)d_in[6]; a.logdt = (const u16*)d_in[7];
  a.x = (const u16*)d_in[0]; a.wg = (const u16*)d_in[10]; a.wu = (const u16*)d_in[11]; a.wd = (const u16*)d_in[12];
  a.gw = (const u16*)d_in[13]; a.ws = (char*)d_ws;
  P2 b; b.dskip = (const u16*)d_in[8]; b.out = (u16*)d_out; b.ws = (char*)d_ws;
  hipFuncSetAttribute((const void*)k1, hipFuncAttributeMaxDynamicSharedMemorySize, 81920);
  hipFuncSetAttribute((const void*)k2, hipFuncAttributeMaxDynamicSharedMemorySize, K2_LDS);
  hipLaunchKernelGGL(k1, dim3(480), dim3(512), 81920, stream, a);
  hipLaunchKernelGGL(k2, dim3(512), dim3(256), K2_LDS, stream, b);
}
```

```cpp
#include <hip/hip_runtime.h>
#include <hip/hip_bf16.h>
#include <stdint.h>

#if defined(__HIP_DEVICE_COMPILE__)
#pragma clang attribute push(__attribute__((target("no-packed-fp32-ops"))), apply_to = function)
#endif
typedef unsigned short u16;
typedef __attribute__((ext_vector_type(4))) short s16x4;
typedef __attribute__((ext_vector_type(8))) short s16x8;
typedef __attribute__((ext_vector_type(8))) __bf16 bf16x8;
typedef __attribute__((ext_vector_type(2))) __bf16 bf16x2;
typedef __attribute__((ext_vector_type(8))) _Float16 f16x8;
typedef __attribute__((ext_vector_type(2))) _Float16 f16x2;
typedef __attribute__((ext_vector_type(16))) float f32x16;
typedef __attribute__((ext_vector_type(4))) float f32x4;
typedef __attribute__((ext_vector_type(2))) float f32x2;
typedef __attribute__((ext_vector_type(4))) unsigned u32x4;
typedef __attribute__((ext_vector_type(2))) unsigned u32x2;
#define LDS __attribute__((address_space(3)))
#define GLB __attribute__((address_space(1)))

constexpr int L_ = 2048, D_ = 512, N_ = 128, F_ = 1792;
constexpr int KS2 = F_ / 16;
constexpr int KROW = 2080;
constexpr size_t WS_H    = 0;
constexpr size_t WS_WD   = WS_H + (size_t)64 * KS2 * 1024;
constexpr size_t WS_UT   = WS_WD + (size_t)16 * KS2 * 1024;
constexpr size_t WS_KR   = WS_UT + (size_t)D_ * L_ * 2;
constexpr size_t WS_AT   = WS_KR + (size_t)D_ * KROW * 2 + 4096;
constexpr size_t WS_G    = WS_AT + (size_t)D_ * L_ * 2;
constexpr unsigned WS_BYTES = (unsigned)(WS_G + (size_t)L_ * 4);

__device__ __forceinline__ float u2f(unsigned v) { return __builtin_bit_cast(float, v); }
__device__ __forceinline__ unsigned f2u(float v) { return __builtin_bit_cast(unsigned, v); }
__device__ __forceinline__ void syncthreads() {
  __builtin_amdgcn_fence(__ATOMIC_RELEASE, "workgroup"); __builtin_amdgcn_s_barrier(); __builtin_amdgcn_fence(__ATOMIC_ACQUIRE, "workgroup"); }
__device__ __forceinline__ float shx(float v, int lane, int m) {
  return u2f((unsigned)__builtin_amdgcn_ds_bpermute((lane ^ m) << 2, (int)f2u(v))); }
__device__ __forceinline__ float bf2f(u16 v) { return u2f((unsigned)v << 16); }
__device__ __forceinline__ unsigned pk_bf16(float a, float b) {
  f32x2 t = {a, b}; bf16x2 r = __builtin_convertvector(t, bf16x2); return __builtin_bit_cast(unsigned, r); }
__device__ __forceinline__ float bflo(unsigned p) { return u2f(p << 16); }
__device__ __forceinline__ float bfhi(unsigned p) { return u2f(p & 0xffff0000u); }
__device__ __forceinline__ float bfr(float a) { return bflo(pk_bf16(a, 0.f)); }
__device__ __forceinline__ unsigned pk_f16(float a, float b) {
  f32x2 t = {a, b}; f16x2 r = __builtin_convertvector(t, f16x2); return __builtin_bit_cast(unsigned, r); }
__device__ __forceinline__ float sigmoidf_(float x) { return __builtin_amdgcn_rcpf(1.f + __builtin_amdgcn_exp2f(-1.44269504089f * x)); }
#define WAIT_V(n) asm volatile("s_waitcnt vmcnt(" #n ")" ::: "memory")
#define WAIT_L(n) asm volatile("s_waitcnt lgkmcnt(" #n ")" ::: "memory")
#define BAR() __builtin_amdgcn_s_barrier()
#define SCHED() __builtin_amdgcn_sched_barrier(0)
#define ST(ws, row, i)
__device__ __forceinline__ void glds16(const void* g, LDS char* l) {
  __builtin_amdgcn_global_load_lds((const GLB void*)g, (LDS void*)l, 16, 0, 0); }

__device__ __forceinline__ void st16wt(char* ws, unsigned off, u32x4 v) {
  __builtin_amdgcn_raw_buffer_store_b128(v, __builtin_amdgcn_make_buffer_rsrc(ws, 0, WS_BYTES, 0x00020000), off, 0, 16); }
__device__ __forceinline__ s16x4 trread(LDS char* p) { return __builtin_amdgcn_ds_read_tr16_b64_v4i16((LDS s16x4*)p); }
__device__ __forceinline__ bf16x8 cat8(s16x4 a, s16x4 b) {
  s16x8 r = __builtin_shufflevector(a, b, 0, 1, 2, 3, 4, 5, 6, 7); return __builtin_bit_cast(bf16x8, r); }

struct P1 {
  const u16 *u, *are, *aim, *bre, *bim, *cre, *cim, *logdt, *wg, *wu, *wd, *gw;
  char* ws;
};

#define DSR_TR(dst, addr, off) asm volatile("ds_read_b64_tr_b16 %0, %1 offset:%2" : "=v"(dst) : "v"(addr), "i"(off))
#define DSR_128(dst, addr, off) asm volatile("ds_read_b128 %0, %1 offset:%2" : "=v"(dst) : "v"(addr), "i"(off))
__device__ __forceinline__ void dma16(unsigned voff, const char* sbase, unsigned ldsb) {
  asm volatile("s_mov_b32 m0, %2\n\ts_nop 0\n\tglobal_load_lds_dwordx4 %0, %1" :: "v"(voff), "s"(sbase), "s"(ldsb) : "memory"); }
__device__ __forceinline__ void dma4(unsigned voff, const char* sbase, unsigned ldsb) {
  asm volatile("s_mov_b32 m0, %2\n\ts_nop 0\n\tglobal_load_lds_dword %0, %1" :: "v"(voff), "s"(sbase), "s"(ldsb) : "memory"); }
struct Frag { s16x4 a0l, a0h, a1l, a1h; bf16x8 b0, b1; };
#define FRAG_WAIT(f) asm volatile("s_waitcnt lgkmcnt(0)" : "+v"(f.a0l), "+v"(f.a0h), "+v"(f.a1l), "+v"(f.a1h), "+v"(f.b0), "+v"(f.b1) :: "memory")
#define MFMA32(a, b, c) __builtin_amdgcn_mfma_f32_32x32x16_bf16(a, b, c, 0, 0, 0)
struct G1 {
  unsigned a0, a1, bx[4];
  unsigned vg, vu, vx0, vx1;
  const char *wg, *wu, *x;
  unsigned lw, lx;
};
template <int S, int KS> __device__ __forceinline__ void g1_rd0(const G1& g, Frag& f) {
  constexpr int wo = (S % 3) * 16384 + KS * 4096, xo = ((S >> 1) & 1) * 16384;
  DSR_TR(f.a0l, g.a0, wo); DSR_TR(f.a0h, g.a0, wo + 1024); DSR_128(f.b0, g.bx[2 * (S & 1) + KS], xo); }
template <int S, int KS> __device__ __forceinline__ void g1_rd1(const G1& g, Frag& f) {
  constexpr int wo = (S % 3) * 16384 + KS * 4096, xo = ((S >> 1) & 1) * 16384;
  DSR_128(f.b1, g.bx[2 * (S & 1) + KS], xo + 4096); DSR_TR(f.a1l, g.a1, wo); DSR_TR(f.a1h, g.a1, wo + 1024); }
template <int S> __device__ __forceinline__ void g1_issueW(const G1& g) {
  dma16(g.vg, g.wg + (size_t)S * 32 * F_ * 2, g.lw + (S % 3) * 16384);
  dma16(g.vu, g.wu + (size_t)S * 32 * F_ * 2, g.lw + (S % 3) * 16384 + 8192); }
template <int J> __device__ __forceinline__ void g1_issueX(const G1& g) {
  dma16(g.vx0, g.x + J * 128, g.lx + (J & 1) * 16384);
  dma16(g.vx1, g.x + J * 128, g.lx + (J & 1) * 16384 + 1024); }
template <int S> __device__ __forceinline__ void g1_stage(const G1& g, Frag& f0, Frag& f1, f32x16 (&acc)[2][2]) {
  FRAG_WAIT(f0);
  {
    const bf16x8 a0 = cat8(f0.a0l, f0.a0h), a1 = cat8(f0.a1l, f0.a1h);
    acc[0][0] = MFMA32(a0, f0.b0, acc[0][0]); SCHED();
    g1_rd0<S, 1>(g, f1); SCHED();
    acc[0][1] = MFMA32(a0, f0.b1, acc[0][1]); SCHED();
    g1_rd1<S, 1>(g, f1); SCHED();
    acc[1][0] = MFMA32(a1, f0.b0, acc[1][0]); SCHED();
    acc[1][1] = MFMA32(a1, f0.b1, acc[1][1]); SCHED();
  }
  FRAG_WAIT(f1);
  if constexpr (S < 15) {
    if constexpr (S >= 14) { WAIT_V(0); } else if constexpr ((S & 1) == 0) { WAIT_V(4); } else { WAIT_V(2); }
    BAR();
  }
  {
    const bf16x8 a0 = cat8(f1.a0l, f1.a0h), a1 = cat8(f1.a1l, f1.a1h);
    acc[0][0] = MFMA32(a0, f1.b0, acc[0][0]); SCHED();
    if constexpr (S < 15) g1_rd0<S + 1, 0>(g, f0);
    SCHED();
    acc[0][1] = MFMA32(a0, f1.b1, acc[0][1]); SCHED();
    if constexpr (S < 15) g1_rd1<S + 1, 0>(g, f0);
    SCHED();
    acc[1][0] = MFMA32(a1, f1.b0, acc[1][0]); SCHED();
    if constexpr (S + 3 < 16) g1_issueW<S + 3>(g);
    SCHED();
    acc[1][1] = MFMA32(a1, f1.b1, acc[1][1]); SCHED();
    if constexpr ((S & 1) == 1 && (S >> 1) + 2 < 8) g1_issueX<(S >> 1) + 2>(g);
    SCHED();
  }
  if constexpr (S < 15) g1_stage<S + 1>(g, f0, f1, acc);
}
__device__ __forceinline__ void gemm1(const P1& p, LDS char* sm, int b) {
  const int tid = threadIdx.x, w = __builtin_amdgcn_readfirstlane(tid >> 6), l = tid & 63;
  const int xcd = b & 7, idx = b >> 3;
  const int mt = 4 * (xcd >> 1) + (idx & 3), nt = 7 * (xcd & 1) + (idx >> 2);
  const int wm = w & 1, wn = w >> 1;
  const unsigned smb = (unsigned)(size_t)sm;
  G1 g;
  {
    const int krow = 4 * w + (l >> 4);
    g.vg = 2u * (krow * F_ + 8 * ((l & 15) ^ (2 * (l >> 4))));
    g.vu = 2u * (krow * F_ + 8 * ((l & 15) ^ (2 * ((l >> 4) | 4))));
    const int xr0 = 16 * w + (l >> 3);
    g.vx0 = 2u * (xr0 * D_ + 8 * ((l & 7) ^ ((xr0 >> 1) & 7)));
    g.vx1 = 2u * ((xr0 + 8) * D_ + 8 * ((l & 7) ^ (((xr0 + 8) >> 1) & 7)));
    g.wg = (const char*)(p.wg + nt * 128); g.wu = (const char*)(p.wu + nt * 128); g.x = (const char*)(p.u + (size_t)mt * 128 * D_);
    g.lw = smb + w * 1024; g.lx = smb + 49152 + w * 2048;
    const int h = l >> 5, blk = (l >> 4) & 1, q = (l & 15) >> 2, pp = l & 3, img = pp >> 1;
    g.a0 = smb + img * 8192 + (8 * h + q) * 256 + 32 * ((2 * wn) ^ (q | (img << 2))) + 16 * blk + 8 * (pp & 1);
    g.a1 = g.a0 ^ 32;
    const int xrow = 64 * wm + (l & 31);
    const unsigned bb = 49152 + xrow * 128 + 16 * (h ^ ((xrow >> 1) & 7));
#pragma unroll
    for (int c = 0; c < 4; ++c) g.bx[c] = smb + (bb ^ (c * 32));
  }
  f32x16 acc[2][2];
#pragma unroll
  for (int i = 0; i < 2; ++i)
#pragma unroll
    for (int j = 0; j < 2; ++j)
#pragma unroll
      for (int r = 0; r < 16; ++r) acc[i][j][r] = 0.f;
  Frag f0, f1;
  ST(p.ws, b, 0);
#define G1PRIO 2
  __builtin_amdgcn_s_setprio(G1PRIO);
  g1_issueX<0>(g); g1_issueW<0>(g); g1_issueW<1>(g); g1_issueX<1>(g); g1_issueW<2>(g);
  WAIT_V(6); BAR();
  ST(p.ws, b, 1);
  g1_rd0<0, 0>(g, f0); g1_rd1<0, 0>(g, f0);
  g1_stage<0>(g, f0, f1, acc);
  ST(p.ws, b, 2);
#pragma unroll
  for (int i = 0; i < 2; ++i)
#pragma unroll
    for (int j = 0; j < 2; ++j) {
      unsigned o[4];
#pragma unroll
      for (int hh = 0; hh < 2; ++hh) {
        float hv[4];
#pragma unroll
        for (int e = 0; e < 4; e += 2) {
          unsigned gp = pk_bf16(acc[i][j][8 * hh + e], acc[i][j][8 * hh + e + 1]);
          unsigned up = pk_bf16(acc[i][j][8 * hh + 4 + e], acc[i][j][8 * hh + 4 + e + 1]);
          float g0 = bflo(gp), g1 = bfhi(gp);
          unsigned sp = pk_bf16(g0 * sigmoidf_(g0), g1 * sigmoidf_(g1));
          hv[e] = bflo(sp) * bflo(up); hv[e + 1] = bfhi(sp) * bfhi(up);
        }
        o[2 * hh] = pk_bf16(hv[0], hv[1]); o[2 * hh + 1] = pk_bf16(hv[2], hv[3]);
      }
      const int tb = 4 * mt + 2 * wm + j, ks = 8 * nt + 2 * wn + i;
      u32x4 ov = {o[0], o[1], o[2], o[3]};
      st16wt(p.ws, (unsigned)WS_H + (unsigned)((tb * KS2 + ks) * 1024 + l * 16), ov);
    }
  ST(p.ws, b, 3);
}

constexpr int PP_P = 0;
constexpr int PP_Q = PP_P + 2 * 41 * 528;
constexpr int PP_KC = PP_Q + 2 * 16 * 528;
constexpr int PP_T = PP_KC + 2 * 656 * 4;
constexpr int PP_W = PP_T + 64 * 132;
constexpr int PP_S = PP_W + 7 * 1024;
static_assert(PP_S + 64 + 256 <= 81920, "lds");
static_assert(PP_W % 16 == 0 && PP_KC % 16 == 0 && PP_T % 16 == 0, "al");

__device__ __forceinline__ void cmul(float ar, float ai, float br, float bi, float& r, float& i) {
  r = ar * br - ai * bi; i = ar * bi + ai * br; }

#define TS0 3
#define TSLP 9
template <int CTRL, int RM> __device__ __forceinline__ float dppf(float v) {
  return u2f((unsigned)__builtin_amdgcn_update_dpp(0, (int)f2u(v), CTRL, RM, 0xf, true)); }
__device__ __forceinline__ float wsum63(float v) {
  v += dppf<0x111, 0xf>(v); v += dppf<0x112, 0xf>(v); v += dppf<0x114, 0xf>(v); v += dppf<0x118, 0xf>(v);
  v += dppf<0x142, 0xa>(v); v += dppf<0x143, 0xc>(v); return v; }
__device__ __forceinline__ float wmax63(float v) {
  v = fmaxf(v, dppf<0x111, 0xf>(v)); v = fmaxf(v, dppf<0x112, 0xf>(v)); v = fmaxf(v, dppf<0x114, 0xf>(v)); v = fmaxf(v, dppf<0x118, 0xf>(v));
  v = fmaxf(v, dppf<0x142, 0xa>(v)); v = fmaxf(v, dppf<0x143, 0xc>(v)); return v; }
__device__ __forceinline__ void dma4v(const void* g, unsigned ldsb) {
  asm volatile("s_mov_b32 m0, %1\n\ts_nop 0\n\tglobal_load_lds_dword %0, off" :: "v"(g), "s"(ldsb) : "memory"); }

__device__ __forceinline__ void prep(const P1& p, LDS char* sm, int pb) {
  const int tid = threadIdx.x, w = __builtin_amdgcn_readfirstlane(tid >> 6), l = tid & 63;
  const int lt = pb >> 3, dtile = pb & 7;
  ST(p.ws, 224 + pb, 0);
  if (w < 4) {
    const int ch = w >> 1, n = tid & 127, d = 2 * pb + ch, o = d * N_ + n;
    const float are = bf2f(p.are[o]), aim = bf2f(p.aim[o]);
    const float bre = bf2f(p.bre[o]), bim = bf2f(p.bim[o]), cre = bf2f(p.cre[o]), cim = bf2f(p.cim[o]);
    const float dt = bfr(expf(bf2f(p.logdt[d])));
    const float zr = dt * are, zi = dt * aim;
    float wr, wi;
    cmul(cre, cim, dt * bre, dt * bim, wr, wi);
    const float m = wmax63(fmaxf(fabsf(wr), fabsf(wi)));
    int ex = (int)((unsigned)__builtin_amdgcn_readlane((int)f2u(m), 63) >> 23) & 255;
    ex = ex < 2 ? 2 : (ex > 250 ? 250 : ex);
    const float sc = u2f((unsigned)(253 - ex) << 23), isc = u2f((unsigned)(ex + 1) << 23);
    wr *= sc; wi *= sc;
    if (l == 0) *(LDS float*)(sm + PP_S + 4 * w) = isc;
    const float zl = zr * 1.44269504089f, rv = zi * 0.15915494309f;
    auto E = [&](float x, float& er, float& ei) {
      float mag = __builtin_amdgcn_exp2f(zl * x), ph = __builtin_amdgcn_fractf(rv * x);
      er = mag * __builtin_amdgcn_cosf(ph); ei = mag * __builtin_amdgcn_sinf(ph); };
    LDS char* Pb = sm + PP_P + ch * (41 * 528) + n * 4;
    LDS char* Qb = sm + PP_Q + ch * (16 * 528) + n * 4;
    auto stP = [&](int row, float r, float i) { *(LDS unsigned*)(Pb + row * 528) = pk_f16(r, i); };
    auto stQ = [&](int row, float r, float i) { *(LDS unsigned*)(Qb + row * 528) = pk_f16(r, -i); };
    {
      float qr[16], qi[16];
      qr[0] = 1.f; qi[0] = 0.f; E(8.f, qr[1], qi[1]);
#pragma unroll
      for (int j = 2; j < 16; ++j) cmul(qr[j / 2], qi[j / 2], qr[j - j / 2], qi[j - j / 2], qr[j], qi[j]);
#pragma unroll
      for (int j = 0; j < 16; ++j) stQ(j, qr[j], qi[j]);
    }
    float e128r, e128i, e256r, e256i, e512r, e512i, e1kr, e1ki, e2kr, e2ki;
    E(128.f, e128r, e128i); E(256.f, e256r, e256i); E(512.f, e512r, e512i); E(1024.f, e1kr, e1ki); E(2048.f, e2kr, e2ki);
    float ar[16], ai[16];
    {
      float er[8], ei[8];
      er[0] = 1.f; ei[0] = 0.f; E(1.f, er[1], ei[1]);
#pragma unroll
      for (int j = 2; j < 8; ++j) cmul(er[j / 2], ei[j / 2], er[j - j / 2], ei[j - j / 2], er[j], ei[j]);
      ar[0] = wr; ai[0] = wi;
#pragma unroll
      for (int j = 1; j < 8; ++j) cmul(wr, wi, er[j], ei[j], ar[j], ai[j]);
#pragma unroll
      for (int j = 0; j < 8; ++j) cmul(ar[j], ai[j], e128r, e128i, ar[8 + j], ai[8 + j]);
#pragma unroll
      for (int j = 0; j < 16; ++j) stP(j, ar[j], ai[j]);
    }
    float br[8], bi[8];
#pragma unroll
    for (int j = 0; j < 8; ++j) { cmul(ar[2 * (j & 3) + 8 * (j >> 2)], ai[2 * (j & 3) + 8 * (j >> 2)], e256r, e256i, br[j], bi[j]); stP(16 + j, br[j], bi[j]); }
    float cr[8], ci[8];
#pragma unroll
    for (int j = 0; j < 8; ++j) {
      const int e = j & 1, ip = j >> 1;
      float sr = ip < 2 ? ar[4 * e + 8 * ip] : br[2 * e + 4 * (ip - 2)], si = ip < 2 ? ai[4 * e + 8 * ip] : bi[2 * e + 4 * (ip - 2)];
      cmul(sr, si, e512r, e512i, cr[j], ci[j]); stP(24 + j, cr[j], ci[j]); }
#pragma unroll
    for (int j = 0; j < 8; ++j) {
      float sr = j < 2 ? ar[8 * j] : (j < 4 ? br[4 * (j - 2)] : cr[2 * (j - 4)]);
      float si = j < 2 ? ai[8 * j] : (j < 4 ? bi[4 * (j - 2)] : ci[2 * (j - 4)]);
      float r, i; cmul(sr, si, e1kr, e1ki, r, i); stP(32 + j, r, i); }
    { float r, i; cmul(wr, wi, e2kr, e2ki, r, i); stP(40, r, i); }
  } else {
    const int t = tid - 256, wv = w - 4;
    const unsigned scr = (unsigned)(size_t)sm + PP_S + 64;
    const u16* tw = ((l & 31) < 14 ? p.wg : p.wu) + (size_t)(lt * F_ + 896 * (pb & 1) + ((l & 31) < 14 ? (l & 31) : (l & 31) - 14) * 64);
    if (wv < 2) { if ((l & 31) < 28) dma4v(tw + (size_t)(32 * (TS0 + 2 * wv + (l >> 5))) * F_, scr); }
    else if (wv == 2) dma4(2u * ((512 * ((pb & 7) >> 1) + 16 * lt + (l >> 3)) * D_) + (l & 7) * 128, (const char*)p.u, scr);
    else dma4(2u * ((512 * ((pb & 7) >> 1) + 16 * lt + 8 + (l >> 3)) * D_) + (l & 7) * 128, (const char*)p.u, scr);
    const u32x4 ut0 = *(const u32x4*)(p.u + (size_t)(64 * lt + (t >> 3)) * D_ + 64 * dtile + 8 * (t & 7));
    const u32x4 ut1 = *(const u32x4*)(p.u + (size_t)(64 * lt + 32 + (t >> 3)) * D_ + 64 * dtile + 8 * (t & 7));
#pragma unroll
    for (int k = 0; k < 2; ++k) {
      const int i = wv + 4 * k, f = 7 * pb + i;
      if (i < 7) glds16(p.wd + (size_t)(16 * (f >> 4) + (l >> 2)) * D_ + 32 * (f & 15) + 8 * (l & 3), sm + PP_W + i * 1024);
    }
    const u32x4 gwv = *(const u32x4*)(p.gw + 8 * l);
    const u32x4 gx0 = *(const u32x4*)(p.u + (size_t)(8 * pb + 2 * wv) * D_ + 8 * l);
    const u32x4 gx1 = *(const u32x4*)(p.u + (size_t)(8 * pb + 2 * wv + 1) * D_ + 8 * l);
    {
      const u32x4 ones = {~0u, ~0u, ~0u, ~0u};
      st16wt(p.ws, (unsigned)WS_AT + (unsigned)(pb * 8192 + t * 16), ones);
      st16wt(p.ws, (unsigned)WS_AT + (unsigned)(pb * 8192 + 4096 + t * 16), ones);
    }
    float s0 = 0.f, s1 = 0.f;
#pragma unroll
    for (int e = 0; e < 4; ++e) {
      const float gl = bflo(gwv[e]), gh = bfhi(gwv[e]);
      unsigned p0 = pk_bf16(bflo(gx0[e]) * gl, bfhi(gx0[e]) * gh), p1 = pk_bf16(bflo(gx1[e]) * gl, bfhi(gx1[e]) * gh);
      s0 += bflo(p0); s0 += bfhi(p0); s1 += bflo(p1); s1 += bfhi(p1);
    }
    s0 = wsum63(s0); s1 = wsum63(s1);
    if (l == 63) {
      f32x2 gg = {bfr(sigmoidf_(bfr(s0))), bfr(sigmoidf_(bfr(s1)))};
      *(f32x2*)(p.ws + WS_G + (8 * pb + 2 * wv) * 4) = gg;
    }
#pragma unroll
    for (int e = 0; e < 4; ++e) {
      *(LDS unsigned*)(sm + PP_T + (t >> 3) * 132 + (t & 7) * 16 + 4 * e) = ut0[e];
      *(LDS unsigned*)(sm + PP_T + (32 + (t >> 3)) * 132 + (t & 7) * 16 + 4 * e) = ut1[e];
    }
    if (wv == 3) {
      for (int s = TS0 + 4; s < 16; ++s) {
        if (l < 28) dma4v(tw + (size_t)(32 * s) * F_, scr);
        __builtin_amdgcn_s_sleep(TSLP);
      }
    }
    WAIT_V(0);
  }
  ST(p.ws, 224 + pb, 1);
  syncthreads();
  if (w < 6) {
    const int c2 = w >= 3, T = w - 3 * c2;
    LDS char* pa = sm + PP_P + c2 * (41 * 528) + (16 * T + (l & 15)) * 528 + (l >> 4) * 16;
    LDS char* pq = sm + PP_Q + c2 * (16 * 528) + (l & 15) * 528 + (l >> 4) * 16;
    f32x4 acc0 = {0.f, 0.f, 0.f, 0.f}, acc1 = {0.f, 0.f, 0.f, 0.f};
#pragma unroll
    for (int s = 0; s < 4; ++s) {
      acc0 = __builtin_amdgcn_mfma_f32_16x16x32_f16(*(LDS f16x8*)(pa + s * 64), *(LDS f16x8*)(pq + s * 64), acc0, 0, 0, 0);
      acc1 = __builtin_amdgcn_mfma_f32_16x16x32_f16(*(LDS f16x8*)(pa + 256 + s * 64), *(LDS f16x8*)(pq + 256 + s * 64), acc1, 0, 0, 0);
    }
    const float is0 = *(LDS float*)(sm + PP_S + 8 * c2), is1 = *(LDS float*)(sm + PP_S + 8 * c2 + 4);
    LDS float* kc = (LDS float*)(sm + PP_KC) + c2 * 656;
    const int j = l & 15;
#pragma unroll
    for (int r = 0; r < 4; ++r) {
      const int row = 4 * (l >> 4) + r;
      int ci_;
      if (T == 0) ci_ = (row & 7) + 128 * (row >> 3) + 8 * j;
      else if (T == 1) ci_ = row < 8 ? 256 + (row & 3) + 64 * (row >> 2) + 4 * j : 384 + (row & 1) + 32 * ((row - 8) >> 1) + 2 * j;
      else ci_ = row < 8 ? 512 + 16 * row + j : ((row == 8 && j == 0) ? 640 : 641);
      kc[ci_] = acc0[r] * is0 + acc1[r] * is1;
    }
  }
  if (w >= 4) {
    const int t = tid - 256;
    if (w >= 6) {
      const int h = l >> 5, blk = (l >> 4) & 1, q = (l & 15) >> 2, pp = l & 3;
#pragma unroll
      for (int k = 0; k < 4; ++k) {
        const int i = 4 * (w - 6) + k, f = 7 * pb + i;
        if (i < 7) {
          LDS char* b0 = sm + PP_W + i * 1024 + (4 * h + q) * 64 + (16 * blk + 4 * pp) * 2;
          s16x4 a = trread(b0), b = trread(b0 + 8 * 64);
          s16x8 r = __builtin_shufflevector(a, b, 0, 1, 2, 3, 4, 5, 6, 7);
          st16wt(p.ws, (unsigned)WS_WD + (unsigned)(((f & 15) * KS2 + (f >> 4)) * 1024 + l * 16), __builtin_bit_cast(u32x4, r));
        }
      }
    }
#pragma unroll
    for (int k = 0; k < 2; ++k) {
      const int dd = (t >> 3) + 32 * k, lc = (t & 7) * 8;
      u32x4 o4;
#pragma unroll
      for (int e = 0; e < 4; ++e) {
        u16 a = *(LDS u16*)(sm + PP_T + (lc + 2 * e) * 132 + 2 * dd), b = *(LDS u16*)(sm + PP_T + (lc + 2 * e + 1) * 132 + 2 * dd);
        o4[e] = (unsigned)a | ((unsigned)b << 16);
      }
      st16wt(p.ws, (unsigned)WS_UT + (unsigned)(((64 * dtile + dd) * L_ + 64 * lt + lc) * 2), o4);
    }
  }
  syncthreads();
  ST(p.ws, 224 + pb, 2);
  {
    const int c2 = w >> 2, c = tid & 255, k = 255 - c, cls = w & 3;
    LDS float* kc = (LDS float*)(sm + PP_KC) + c2 * 656;
    u32x4 ov;
    if (cls < 2) {
      const float lo = kc[384 + k], hi = kc[385 + k];
      const unsigned hh = pk_bf16(hi, hi), ll = pk_bf16(lo, lo);
      ov[0] = hh; ov[1] = (k & 1) ? hh : ((hh & 0xffffu) | (ll & 0xffff0000u)); ov[2] = ll; ov[3] = ll;
    } else if (cls == 2) {
      const float x0 = kc[256 + 2 * k], x1 = kc[257 + 2 * k], x2 = kc[258 + 2 * k];
      ov[0] = pk_bf16(x2, x2); ov[1] = pk_bf16(x1, x1); ov[2] = pk_bf16(x1, x0); ov[3] = pk_bf16(x0, x0);
    } else {
      const bool c_ = l < 32;
      LDS float* b = kc + (c_ ? 128 + 4 * k : 8 * k);
      const float v0 = b[c_ ? 4 : 7], v1 = b[c_ ? 3 : 6], v2 = b[c_ ? 2 : 5], v3 = b[c_ ? 2 : 4];
      const float v4 = b[c_ ? 2 : 3], v5 = b[c_ ? 1 : 2], v6 = b[c_ ? 0 : 1], v7 = b[0];
      ov[0] = pk_bf16(v0, v1); ov[1] = pk_bf16(v2, v3); ov[2] = pk_bf16(v4, v5); ov[3] = pk_bf16(v6, v7);
    }
    st16wt(p.ws, (unsigned)WS_KR + (unsigned)(((2 * pb + c2) * KROW + 8 * c) * 2), ov);
    if (tid < 8) { u32x4 z = {0u, 0u, 0u, 0u}; st16wt(p.ws, (unsigned)WS_KR + (unsigned)(((2 * pb + (tid >> 2)) * KROW + 2048 + 8 * (tid & 3)) * 2), z); }
  }
  ST(p.ws, 224 + pb, 3);
}

__global__ void __launch_bounds__(512, 4) k1(P1 p) {
  extern __shared__ __attribute__((aligned(16))) char smem[];
  LDS char* sm = (LDS char*)smem;
  if (blockIdx.x < 224) gemm1(p, sm, blockIdx.x); else prep(p, sm, blockIdx.x - 224);
}

struct P2 { const u16* dskip; u16* out; char* ws; };
constexpr int CV_K = 0;
constexpr int CV_U = 2 * 4 * 4160;
constexpr int CV_R = CV_U + 2 * 4608;
constexpr int K2_LDS = 65536 + 8192;

__device__ __forceinline__ void conv(const P2& p, LDS char* sm, int cb) {
  const int tid = threadIdx.x, w = __builtin_amdgcn_readfirstlane(tid >> 6), l = tid & 63;
  ST(p.ws, 512 + cb, 0);
  __builtin_amdgcn_s_setprio(3);
  const u16* ut = (const u16*)(p.ws + WS_UT) + (size_t)(2 * cb) * L_;
  const u16* kr = (const u16*)(p.ws + WS_KR) + (size_t)(2 * cb) * KROW;
  u32x4 uv[2];
#pragma unroll
  for (int c = 0; c < 2; ++c) uv[c] = *(const u32x4*)(ut + c * L_ + 8 * tid);
  u32x4 k0[3]; u32x2 k1v[3];
#pragma unroll
  for (int i = 0; i < 3; ++i) {
    int job = tid + 256 * i; job = job < 520 ? job : 519;
    const int c2 = job >= 260, c = job - 260 * c2;
    k0[i] = *(const u32x4*)(kr + c2 * KROW + 8 * c);
    k1v[i] = *(const u32x2*)(kr + c2 * KROW + 8 * c + 8);
    if (c == 259) { k1v[i][0] = 0u; k1v[i][1] = 0u; }
  }
  const float dsk = bf2f(p.dskip[2 * cb + (w >> 1)]);
#pragma unroll
  for (int c = 0; c < 2; ++c) {
    *(LDS u32x4*)(sm + CV_U + c * 4608 + 512 + 16 * tid) = uv[c];
    if (tid < 32) { u32x4 z = {0u, 0u, 0u, 0u}; *(LDS u32x4*)(sm + CV_U + c * 4608 + 16 * tid) = z; }
  }
#pragma unroll
  for (int i = 0; i < 3; ++i) {
    const int job = tid + 256 * i;
    if (job < 520) {
      const int c2 = job >= 260, c = job - 260 * c2;
      unsigned dd[6] = {k0[i][0], k0[i][1], k0[i][2], k0[i][3], k1v[i][0], k1v[i][1]};
      LDS char* b = sm + CV_K + c2 * (4 * 4160) + 16 * c;
      u32x4 c0 = {dd[0], dd[1], dd[2], dd[3]}, c2v = {dd[1], dd[2], dd[3], dd[4]}, c1, c3;
#pragma unroll
      for (int e = 0; e < 4; ++e) { c1[e] = __builtin_amdgcn_alignbit(dd[e + 1], dd[e], 16); c3[e] = __builtin_amdgcn_alignbit(dd[e + 2], dd[e + 1], 16); }
      *(LDS u32x4*)(b) = c0; *(LDS u32x4*)(b + 4160) = c1; *(LDS u32x4*)(b + 2 * 4160) = c2v; *(LDS u32x4*)(b + 3 * 4160) = c3;
    }
  }
  syncthreads();
  ST(p.ws, 512 + cb, 1);
  const int ch = w >> 1, rh = w & 1, j = l & 15, g = l >> 4;
  const int e = 3 - (j & 3);
  LDS char* ka = sm + CV_K + ch * (4 * 4160) + e * 4160 + 2 * (2031 - j + 8 * g - e);
  LDS char* ub = sm + CV_U + ch * 4608 + 2 * (240 + 16 * j + 8 * g);
  f32x4 acc[8];
#pragma unroll
  for (int c = 0; c < 8; ++c) acc[c] = f32x4{0.f, 0.f, 0.f, 0.f};
#pragma unroll
  for (int rr = 0; rr < 4; ++rr) {
    const int r = 4 * rh + rr;
    bf16x8 bf[8];
#pragma unroll
    for (int t = 0; t < 8; ++t) bf[t] = *(LDS bf16x8*)(ub + 512 * t - 64 * r);
#pragma unroll
    for (int q = 0; q < 8; ++q) {
      LDS char* a = ka - 64 * (8 * q + r);
      s16x4 lo = *(LDS s16x4*)a, hi = *(LDS s16x4*)(a + 8);
      bf16x8 af = cat8(lo, hi);
#pragma unroll
      for (int c = q; c < 8; ++c) acc[c] = __builtin_amdgcn_mfma_f32_16x16x32_bf16(af, bf[c - q], acc[c], 0, 0, 0);
    }
  }
  ST(p.ws, 512 + cb, 2);
  LDS char* rb = sm + CV_R;
#pragma unroll
  for (int c = 0; c < 4; ++c) *(LDS f32x4*)(rb + (w * 4 + c) * 1024 + l * 16) = acc[4 * (rh ^ 1) + c];
  syncthreads();
  u16* at = (u16*)(p.ws + WS_AT) + (size_t)(2 * cb + ch) * L_;
#pragma unroll
  for (int c = 0; c < 4; ++c) {
    f32x4 o = *(LDS f32x4*)(rb + ((w ^ 1) * 4 + c) * 1024 + l * 16);
    f32x4 y = acc[4 * rh + c] + o;
    const int lpos = 256 * (4 * rh + c) + 16 * j + 4 * g;
    u32x2 uu = *(LDS u32x2*)(sm + CV_U + ch * 4608 + 512 + 2 * lpos);
    unsigned y0 = pk_bf16(y[0], y[1]), y1 = pk_bf16(y[2], y[3]);
    unsigned s0 = pk_bf16(bflo(uu[0]) * dsk, bfhi(uu[0]) * dsk), s1 = pk_bf16(bflo(uu[1]) * dsk, bfhi(uu[1]) * dsk);
    unsigned a0 = pk_bf16(bflo(y0) + bflo(s0), bfhi(y0) + bfhi(s0)), a1 = pk_bf16(bflo(y1) + bflo(s1), bfhi(y1) + bfhi(s1));
    unsigned long long v = (unsigned long long)a0 | ((unsigned long long)a1 << 32);
    if (v == ~0ull) v = 0x7FC07FC07FC07FC0ull;
    __hip_atomic_store((GLB unsigned long long*)(at + lpos), v, __ATOMIC_RELAXED, __HIP_MEMORY_SCOPE_AGENT);
  }
  ST(p.ws, 512 + cb, 3);
}

#define PFX 9
constexpr int PF = PFX;
struct F2 { bf16x8 a0, a1, b0, b1; };
#define GLD16(dst, voff, sbase, off) asm volatile("global_load_dwordx4 %0, %1, %2 offset:%3" : "=v"(dst) : "v"(voff), "s"(sbase), "i"(off))
template <int S> __device__ __forceinline__ void g2_ld(F2& f, unsigned vo, const char* A0, const char* B0) {
  const char *a = A0 + (S >> 2) * 4096, *b = B0 + (S >> 2) * 4096;
  GLD16(f.a0, vo, a, (S & 3) * 1024); GLD16(f.b0, vo, b, (S & 3) * 1024);
  GLD16(f.a1, vo, a + KS2 * 1024, (S & 3) * 1024); GLD16(f.b1, vo, b + KS2 * 1024, (S & 3) * 1024); }
template <int S> __device__ __forceinline__ void g2_pro(F2 (&f)[PF], unsigned vo, const char* A0, const char* B0) {
  g2_ld<S>(f[S], vo, A0, B0);
  if constexpr (S + 1 < PF) g2_pro<S + 1>(f, vo, A0, B0); }
template <int S> __device__ __forceinline__ void g2_step(F2 (&f)[PF], unsigned vo, const char* A0, const char* B0, f32x16 (&acc)[2][2]) {
  F2& c = f[S % PF];
  constexpr int pend = (S + PF <= 28 ? PF : 28 - S) - 1;
  asm volatile("s_waitcnt vmcnt(%4)" : "+v"(c.a0), "+v"(c.a1), "+v"(c.b0), "+v"(c.b1) : "i"(4 * pend) : "memory");
  acc[0][0] = MFMA32(c.a0, c.b0, acc[0][0]); acc[0][1] = MFMA32(c.a0, c.b1, acc[0][1]);
  acc[1][0] = MFMA32(c.a1, c.b0, acc[1][0]); acc[1][1] = MFMA32(c.a1, c.b1, acc[1][1]);
  SCHED();
  if constexpr (S + PF < 28) g2_ld<S + PF>(c, vo, A0, B0);
  SCHED();
  if constexpr (S + 1 < 28) g2_step<S + 1>(f, vo, A0, B0, acc);
}
__device__ __forceinline__ void gemm2(const P2& p, LDS char* sm, int b) {
  const int tid = threadIdx.x, w = __builtin_amdgcn_readfirstlane(tid >> 6), l = tid & 63;
  const int xcd = b & 7, idx = b >> 3;
  const int mt = 4 * xcd + (idx & 3), nt = idx >> 2;
#define K2NT nt
#define K2MT mt
  const char* A0 = p.ws + WS_WD + ((size_t)(2 * (K2NT)) * KS2 + 28 * w) * 1024;
  const char* B0 = p.ws + WS_H + ((size_t)(2 * (K2MT)) * KS2 + 28 * w) * 1024;
  f32x16 acc[2][2];
#pragma unroll
  for (int i = 0; i < 2; ++i)
#pragma unroll
    for (int j = 0; j < 2; ++j)
#pragma unroll
      for (int r = 0; r < 16; ++r) acc[i][j][r] = 0.f;
  ST(p.ws, 768 + b, 0);
  const int tl = 16 * w + (l & 15), tok = 64 * mt + tl;
  float g;
  asm volatile("global_load_dword %0, %1, %2" : "=v"(g) : "v"(tok * 4), "s"(p.ws + WS_G));
  F2 f[PF];
  g2_pro<0>(f, l * 16, A0, B0);
  SCHED();
  g2_step<0>(f, l * 16, A0, B0, acc);
  asm volatile("s_waitcnt vmcnt(0)" : "+v"(g) :: "memory");
  ST(p.ws, 768 + b, 1);
  GLB unsigned long long* at = (GLB unsigned long long*)((const u16*)(p.ws + WS_AT) + (size_t)(64 * nt + (tid >> 4)) * L_ + 64 * mt + 4 * (tid & 15));
  unsigned long long av8[4];
#pragma unroll
  for (int i = 0; i < 4; ++i) av8[i] = __hip_atomic_load(at + (size_t)i * 16 * L_ / 4, __ATOMIC_RELAXED, __HIP_MEMORY_SCOPE_AGENT);
#pragma unroll
  for (int i = 0; i < 2; ++i)
#pragma unroll
    for (int j = 0; j < 2; ++j)
#pragma unroll
      for (int q = 0; q < 4; ++q) {
        f32x4 v = {acc[i][j][4 * q], acc[i][j][4 * q + 1], acc[i][j][4 * q + 2], acc[i][j][4 * q + 3]};
        *(LDS f32x4*)(sm + w * 16384 + ((2 * i + j) * 4 + q) * 1024 + l * 16) = v;
      }
  for (unsigned spins = 0; spins < (1u << 22); ++spins) {
    const bool bad = av8[0] == ~0ull || av8[1] == ~0ull || av8[2] == ~0ull || av8[3] == ~0ull;
    if (__builtin_amdgcn_ballot_w64(bad) == 0ull) break;
    __builtin_amdgcn_s_sleep(1);
#pragma unroll
    for (int i = 0; i < 4; ++i) av8[i] = __hip_atomic_load(at + (size_t)i * 16 * L_ / 4, __ATOMIC_RELAXED, __HIP_MEMORY_SCOPE_AGENT);
  }
#pragma unroll
  for (int i = 0; i < 4; ++i) *(LDS unsigned long long*)(sm + 65536 + ((tid >> 4) + 16 * i) * 128 + (tid & 15) * 8) = av8[i];
  syncthreads();
  ST(p.ws, 768 + b, 2);
  const float og = bfr(1.f - g);
  const int q = l >> 4, qq = (l & 15) >> 2, pp = l & 3;
#pragma unroll
  for (int k = 0; k < 2; ++k) {
    unsigned ov[4];
#pragma unroll
    for (int h = 0; h < 2; ++h) {
      LDS char* rp = sm + ((2 * k + (w >> 1)) * 4 + q) * 1024 + (16 * (w & 1) + (l & 15) + 32 * h) * 16;
      f32x4 s = *(LDS f32x4*)rp;
#pragma unroll
      for (int ww = 1; ww < 4; ++ww) s += *(LDS f32x4*)(rp + ww * 16384);
      s16x4 av = trread(sm + 65536 + (32 * k + 8 * q + 4 * h + qq) * 128 + (16 * w + 4 * pp) * 2);
      float o[4];
#pragma unroll
      for (int e = 0; e < 4; ++e) o[e] = bfr(g * bf2f((u16)av[e])) + bfr(og * bfr(s[e]));
      ov[2 * h] = pk_bf16(o[0], o[1]); ov[2 * h + 1] = pk_bf16(o[2], o[3]);
    }
    u32x4 o4 = {ov[0], ov[1], ov[2], ov[3]};
    *(u32x4*)(p.out + (size_t)tok * D_ + 64 * nt + 32 * k + 8 * q) = o4;
  }
  ST(p.ws, 768 + b, 3);
}

__global__ void __launch_bounds__(256, 2) k2(P2 p) {
  extern __shared__ __attribute__((aligned(16))) char smem[];
  LDS char* sm = (LDS char*)smem;
  if (blockIdx.x < 256) conv(p, sm, blockIdx.x); else gemm2(p, sm, blockIdx.x - 256);
}

#if defined(__HIP_DEVICE_COMPILE__)
#pragma clang attribute pop
#endif
extern "C" void kernel_launch(void* const* d_in, const int* in_sizes, int n_in, void* d_out, int out_size,
                              void* d_ws, size_t ws_size, hipStream_t stream) {
  P1 a;
  a.u = (const u16*)d_in[0]; a.are = (const u16*)d_in[1]; a.aim = (const u16*)d_in[2]; a.bre = (const u16*)d_in[3];
  a.bim = (const u16*)d_in[4]; a.cre = (const u16*)d_in[5]; a.cim = (const u16*)d_in[6]; a.logdt = (const u16*)d_in[7];
  a.wg = (const u16*)d_in[10]; a.wu = (const u16*)d_in[11]; a.wd = (const u16*)d_in[12];
  a.gw = (const u16*)d_in[13]; a.ws = (char*)d_ws;
  P2 b; b.dskip = (const u16*)d_in[8]; b.out = (u16*)d_out; b.ws = (char*)d_ws;
  (void)hipFuncSetAttribute((const void*)k1, hipFuncAttributeMaxDynamicSharedMemorySize, 81920);
  (void)hipFuncSetAttribute((const void*)k2, hipFuncAttributeMaxDynamicSharedMemorySize, K2_LDS);
  hipLaunchKernelGGL(k1, dim3(480), dim3(512), 81920, stream, a);
  hipLaunchKernelGGL(k2, dim3(512), dim3(256), K2_LDS, stream, b);
}
```

```cpp
#include <hip/hip_runtime.h>
#include <hip/hip_bf16.h>
#include <stdint.h>

#if defined(__HIP_DEVICE_COMPILE__)
#pragma clang attribute push(__attribute__((target("no-packed-fp32-ops"))), apply_to = function)
#endif
typedef unsigned short u16;
typedef __attribute__((ext_vector_type(4))) short s16x4;
typedef __attribute__((ext_vector_type(8))) short s16x8;
typedef __attribute__((ext_vector_type(8))) __bf16 bf16x8;
typedef __attribute__((ext_vector_type(2))) __bf16 bf16x2;
typedef __attribute__((ext_vector_type(8))) _Float16 f16x8;
typedef __attribute__((ext_vector_type(2))) _Float16 f16x2;
typedef __attribute__((ext_vector_type(16))) float f32x16;
typedef __attribute__((ext_vector_type(4))) float f32x4;
typedef __attribute__((ext_vector_type(2))) float f32x2;
typedef __attribute__((ext_vector_type(4))) unsigned u32x4;
typedef __attribute__((ext_vector_type(2))) unsigned u32x2;
#define LDS __attribute__((address_space(3)))
#define GLB __attribute__((address_space(1)))

constexpr int L_ = 2048, D_ = 512, N_ = 128, F_ = 1792;
constexpr int KS2 = F_ / 16;
constexpr int KROW = 2080;
constexpr size_t WS_H    = 0;
constexpr size_t WS_WD   = WS_H + (size_t)64 * KS2 * 1024;
constexpr size_t WS_UT   = WS_WD + (size_t)16 * KS2 * 1024;
constexpr size_t WS_KR   = WS_UT + (size_t)D_ * L_ * 2;
constexpr size_t WS_AT   = WS_KR + (size_t)D_ * KROW * 2 + 4096;
constexpr size_t WS_G    = WS_AT + (size_t)D_ * L_ * 2;
constexpr unsigned WS_BYTES = (unsigned)(WS_G + (size_t)L_ * 4);

__device__ __forceinline__ float u2f(unsigned v) { return __builtin_bit_cast(float, v); }
__device__ __forceinline__ unsigned f2u(float v) { return __builtin_bit_cast(unsigned, v); }
__device__ __forceinline__ void syncthreads() {
  __builtin_amdgcn_fence(__ATOMIC_RELEASE, "workgroup"); __builtin_amdgcn_s_barrier(); __builtin_amdgcn_fence(__ATOMIC_ACQUIRE, "workgroup"); }
__device__ __forceinline__ float shx(float v, int lane, int m) {
  return u2f((unsigned)__builtin_amdgcn_ds_bpermute((lane ^ m) << 2, (int)f2u(v))); }
__device__ __forceinline__ float bf2f(u16 v) { return u2f((unsigned)v << 16); }
__device__ __forceinline__ unsigned pk_bf16(float a, float b) {
  f32x2 t = {a, b}; bf16x2 r = __builtin_convertvector(t, bf16x2); return __builtin_bit_cast(unsigned, r); }
__device__ __forceinline__ float bflo(unsigned p) { return u2f(p << 16); }
__device__ __forceinline__ float bfhi(unsigned p) { return u2f(p & 0xffff0000u); }
__device__ __forceinline__ float bfr(float a) { return bflo(pk_bf16(a, 0.f)); }
__device__ __forceinline__ unsigned pk_f16(float a, float b) {
  f32x2 t = {a, b}; f16x2 r = __builtin_convertvector(t, f16x2); return __builtin_bit_cast(unsigned, r); }
__device__ __forceinline__ float sigmoidf_(float x) { return __builtin_amdgcn_rcpf(1.f + __builtin_amdgcn_exp2f(-1.44269504089f * x)); }
#define WAIT_V(n) asm volatile("s_waitcnt vmcnt(" #n ")" ::: "memory")
#define WAIT_L(n) asm volatile("s_waitcnt lgkmcnt(" #n ")" ::: "memory")
#define BAR() __builtin_amdgcn_s_barrier()
#define SCHED() __builtin_amdgcn_sched_barrier(0)
#define ST(ws, row, i)
__device__ __forceinline__ void glds16(const void* g, LDS char* l) {
  __builtin_amdgcn_global_load_lds((const GLB void*)g, (LDS void*)l, 16, 0, 0); }

__device__ __forceinline__ void st16wt(char* ws, unsigned off, u32x4 v) {
  __builtin_amdgcn_raw_buffer_store_b128(v, __builtin_amdgcn_make_buffer_rsrc(ws, 0, WS_BYTES, 0x00020000), off, 0, 16); }
__device__ __forceinline__ s16x4 trread(LDS char* p) { return __builtin_amdgcn_ds_read_tr16_b64_v4i16((LDS s16x4*)p); }
__device__ __forceinline__ bf16x8 cat8(s16x4 a, s16x4 b) {
  s16x8 r = __builtin_shufflevector(a, b, 0, 1, 2, 3, 4, 5, 6, 7); return __builtin_bit_cast(bf16x8, r); }

struct P1 {
  const u16 *u, *are, *aim, *bre, *bim, *cre, *cim, *logdt, *wg, *wu, *wd, *gw;
  char* ws;
};

#define DSR_TR(dst, addr, off) asm volatile("ds_read_b64_tr_b16 %0, %1 offset:%2" : "=v"(dst) : "v"(addr), "i"(off))
#define DSR_128(dst, addr, off) asm volatile("ds_read_b128 %0, %1 offset:%2" : "=v"(dst) : "v"(addr), "i"(off))
__device__ __forceinline__ void dma16(unsigned voff, const char* sbase, unsigned ldsb) {
  asm volatile("s_mov_b32 m0, %2\n\ts_nop 0\n\tglobal_load_lds_dwordx4 %0, %1" :: "v"(voff), "s"(sbase), "s"(ldsb) : "memory"); }
__device__ __forceinline__ void dma4(unsigned voff, const char* sbase, unsigned ldsb) {
  asm volatile("s_mov_b32 m0, %2\n\ts_nop 0\n\tglobal_load_lds_dword %0, %1" :: "v"(voff), "s"(sbase), "s"(ldsb) : "memory"); }
struct Frag { s16x4 a0l, a0h, a1l, a1h; bf16x8 b0, b1; };
#define FRAG_WAIT(f) asm volatile("s_waitcnt lgkmcnt(0)" : "+v"(f.a0l), "+v"(f.a0h), "+v"(f.a1l), "+v"(f.a1h), "+v"(f.b0), "+v"(f.b1) :: "memory")
#define MFMA32(a, b, c) __builtin_amdgcn_mfma_f32_32x32x16_bf16(a, b, c, 0, 0, 0)
struct G1 {
  unsigned a0, a1, bx[4];
  unsigned vg, vu, vx0, vx1;
  const char *wg, *wu, *x;
  unsigned lw, lx;
};
template <int S, int KS> __device__ __forceinline__ void g1_rd0(const G1& g, Frag& f) {
  constexpr int wo = (S % 3) * 16384 + KS * 4096, xo = ((S >> 1) & 1) * 16384;
  DSR_TR(f.a0l, g.a0, wo); DSR_TR(f.a0h, g.a0, wo + 1024); DSR_128(f.b0, g.bx[2 * (S & 1) + KS], xo); }
template <int S, int KS> __device__ __forceinline__ void g1_rd1(const G1& g, Frag& f) {
  constexpr int wo = (S % 3) * 16384 + KS * 4096, xo = ((S >> 1) & 1) * 16384;
  DSR_128(f.b1, g.bx[2 * (S & 1) + KS], xo + 4096); DSR_TR(f.a1l, g.a1, wo); DSR_TR(f.a1h, g.a1, wo + 1024); }
template <int S> __device__ __forceinline__ void g1_issueW(const G1& g) {
  dma16(g.vg, g.wg + (size_t)S * 32 * F_ * 2, g.lw + (S % 3) * 16384);
  dma16(g.vu, g.wu + (size_t)S * 32 * F_ * 2, g.lw + (S % 3) * 16384 + 8192); }
template <int J> __device__ __forceinline__ void g1_issueX(const G1& g) {
  dma16(g.vx0, g.x + J * 128, g.lx + (J & 1) * 16384);
  dma16(g.vx1, g.x + J * 128, g.lx + (J & 1) * 16384 + 1024); }
template <int S> __device__ __forceinline__ void g1_stage(const G1& g, Frag& f0, Frag& f1, f32x16 (&acc)[2][2]) {
  FRAG_WAIT(f0);
  {
    const bf16x8 a0 = cat8(f0.a0l, f0.a0h), a1 = cat8(f0.a1l, f0.a1h);
    acc[0][0] = MFMA32(a0, f0.b0, acc[0][0]); SCHED();
    g1_rd0<S, 1>(g, f1); SCHED();
    acc[0][1] = MFMA32(a0, f0.b1, acc[0][1]); SCHED();
    g1_rd1<S, 1>(g, f1); SCHED();
    acc[1][0] = MFMA32(a1, f0.b0, acc[1][0]); SCHED();
    acc[1][1] = MFMA32(a1, f0.b1, acc[1][1]); SCHED();
  }
  FRAG_WAIT(f1);
  if constexpr (S < 15) {
    if constexpr (S >= 14) { WAIT_V(0); } else if constexpr ((S & 1) == 0) { WAIT_V(4); } else { WAIT_V(2); }
    BAR();
  }
  {
    const bf16x8 a0 = cat8(f1.a0l, f1.a0h), a1 = cat8(f1.a1l, f1.a1h);
    acc[0][0] = MFMA32(a0, f1.b0, acc[0][0]); SCHED();
    if constexpr (S < 15) g1_rd0<S + 1, 0>(g, f0);
    SCHED();
    acc[0][1] = MFMA32(a0, f1.b1, acc[0][1]); SCHED();
    if constexpr (S < 15) g1_rd1<S + 1, 0>(g, f0);
    SCHED();
    acc[1][0] = MFMA32(a1, f1.b0, acc[1][0]); SCHED();
    if constexpr (S + 3 < 16) g1_issueW<S + 3>(g);
    SCHED();
    acc[1][1] = MFMA32(a1, f1.b1, acc[1][1]); SCHED();
    if constexpr ((S & 1) == 1 && (S >> 1) + 2 < 8) g1_issueX<(S >> 1) + 2>(g);
    SCHED();
  }
  if constexpr (S < 15) g1_stage<S + 1>(g, f0, f1, acc);
}
__device__ __forceinline__ void gemm1(const P1& p, LDS char* sm, int b) {
  const int tid = threadIdx.x, w = __builtin_amdgcn_readfirstlane(tid >> 6), l = tid & 63;
  const int xcd = b & 7, idx = b >> 3;
  const int mt = 4 * (xcd >> 1) + (idx & 3), nt = 7 * (xcd & 1) + (idx >> 2);
  const int wm = w & 1, wn = w >> 1;
  const unsigned smb = (unsigned)(size_t)sm;
  G1 g;
  {
    const int krow = 4 * w + (l >> 4);
    g.vg = 2u * (krow * F_ + 8 * ((l & 15) ^ (2 * (l >> 4))));
    g.vu = 2u * (krow * F_ + 8 * ((l & 15) ^ (2 * ((l >> 4) | 4))));
    const int xr0 = 16 * w + (l >> 3);
    g.vx0 = 2u * (xr0 * D_ + 8 * ((l & 7) ^ ((xr0 >> 1) & 7)));
    g.vx1 = 2u * ((xr0 + 8) * D_ + 8 * ((l & 7) ^ (((xr0 + 8) >> 1) & 7)));
    g.wg = (const char*)(p.wg + nt * 128); g.wu = (const char*)(p.wu + nt * 128); g.x = (const char*)(p.u + (size_t)mt * 128 * D_);
    g.lw = smb + w * 1024; g.lx = smb + 49152 + w * 2048;
    const int h = l >> 5, blk = (l >> 4) & 1, q = (l & 15) >> 2, pp = l & 3, img = pp >> 1;
    g.a0 = smb + img * 8192 + (8 * h + q) * 256 + 32 * ((2 * wn) ^ (q | (img << 2))) + 16 * blk + 8 * (pp & 1);
    g.a1 = g.a0 ^ 32;
    const int xrow = 64 * wm + (l & 31);
    const unsigned bb = 49152 + xrow * 128 + 16 * (h ^ ((xrow >> 1) & 7));
#pragma unroll
    for (int c = 0; c < 4; ++c) g.bx[c] = smb + (bb ^ (c * 32));
  }
  f32x16 acc[2][2];
#pragma unroll
  for (int i = 0; i < 2; ++i)
#pragma unroll
    for (int j = 0; j < 2; ++j)
#pragma unroll
      for (int r = 0; r < 16; ++r) acc[i][j][r] = 0.f;
  Frag f0, f1;
  ST(p.ws, b, 0);
#define G1PRIO 2
  __builtin_amdgcn_s_setprio(G1PRIO);
  g1_issueX<0>(g); g1_issueW<0>(g); g1_issueW<1>(g); g1_issueX<1>(g); g1_issueW<2>(g);
  WAIT_V(6); BAR();
  ST(p.ws, b, 1);
  g1_rd0<0, 0>(g, f0); g1_rd1<0, 0>(g, f0);
  g1_stage<0>(g, f0, f1, acc);
  ST(p.ws, b, 2);
#pragma unroll
  for (int i = 0; i < 2; ++i)
#pragma unroll
    for (int j = 0; j < 2; ++j) {
      unsigned o[4];
#pragma unroll
      for (int hh = 0; hh < 2; ++hh) {
        float hv[4];
#pragma unroll
        for (int e = 0; e < 4; e += 2) {
          unsigned gp = pk_bf16(acc[i][j][8 * hh + e], acc[i][j][8 * hh + e + 1]);
          unsigned up = pk_bf16(acc[i][j][8 * hh + 4 + e], acc[i][j][8 * hh + 4 + e + 1]);
          float g0 = bflo(gp), g1 = bfhi(gp);
          unsigned sp = pk_bf16(g0 * sigmoidf_(g0), g1 * sigmoidf_(g1));
          hv[e] = bflo(sp) * bflo(up); hv[e + 1] = bfhi(sp) * bfhi(up);
        }
        o[2 * hh] = pk_bf16(hv[0], hv[1]); o[2 * hh + 1] = pk_bf16(hv[2], hv[3]);
      }
      const int tb = 4 * mt + 2 * wm + j, ks = 8 * nt + 2 * wn + i;
      u32x4 ov = {o[0], o[1], o[2], o[3]};
      st16wt(p.ws, (unsigned)WS_H + (unsigned)((tb * KS2 + ks) * 1024 + l * 16), ov);
    }
  ST(p.ws, b, 3);
}

constexpr int PP_P = 0;
constexpr int PP_Q = PP_P + 2 * 41 * 528;
constexpr int PP_KC = PP_Q + 2 * 16 * 528;
constexpr int PP_T = PP_KC + 2 * 656 * 4;
constexpr int PP_W = PP_T + 64 * 132;
constexpr int PP_S = PP_W + 7 * 1024;
static_assert(PP_S + 64 + 256 <= 81920, "lds");
static_assert(PP_W % 16 == 0 && PP_KC % 16 == 0 && PP_T % 16 == 0, "al");

__device__ __forceinline__ void cmul(float ar, float ai, float br, float bi, float& r, float& i) {
  r = ar * br - ai * bi; i = ar * bi + ai * br; }

#define TS0 3
#define TSLP 9
#define TSPLIT 3
#define TSLP2 4
#define PP1 3
#define PP2 3
#define CVP 3
#define G2P 0
#define CVR r
#define CVQ q
#define CVM 0
#define CT 1
#define CTB 256
#define CTK2 9216u
#define TW2 7
template <int CTRL, int RM> __device__ __forceinline__ float dppf(float v) {
  return u2f((unsigned)__builtin_amdgcn_update_dpp(0, (int)f2u(v), CTRL, RM, 0xf, true)); }
__device__ __forceinline__ float wsum63(float v) {
  v += dppf<0x111, 0xf>(v); v += dppf<0x112, 0xf>(v); v += dppf<0x114, 0xf>(v); v += dppf<0x118, 0xf>(v);
  v += dppf<0x142, 0xa>(v); v += dppf<0x143, 0xc>(v); return v; }
__device__ __forceinline__ float wmax63(float v) {
  v = fmaxf(v, dppf<0x111, 0xf>(v)); v = fmaxf(v, dppf<0x112, 0xf>(v)); v = fmaxf(v, dppf<0x114, 0xf>(v)); v = fmaxf(v, dppf<0x118, 0xf>(v));
  v = fmaxf(v, dppf<0x142, 0xa>(v)); v = fmaxf(v, dppf<0x143, 0xc>(v)); return v; }
__device__ __forceinline__ void dma4v(const void* g, unsigned ldsb) {
  asm volatile("s_mov_b32 m0, %1\n\ts_nop 0\n\tglobal_load_lds_dword %0, off" :: "v"(g), "s"(ldsb) : "memory"); }

__device__ __forceinline__ void prep(const P1& p, LDS char* sm, int pb) {
  const int tid = threadIdx.x, w = __builtin_amdgcn_readfirstlane(tid >> 6), l = tid & 63;
  const int lt = pb >> 3, dtile = pb & 7;
  ST(p.ws, 224 + pb, 0);
  if (w < 4) {
    __builtin_amdgcn_s_setprio(PP1);
    const int ch = w >> 1, n = tid & 127, d = 2 * pb + ch, o = d * N_ + n;
    const float are = bf2f(p.are[o]), aim = bf2f(p.aim[o]);
    const float bre = bf2f(p.bre[o]), bim = bf2f(p.bim[o]), cre = bf2f(p.cre[o]), cim = bf2f(p.cim[o]);
    const float dt = bfr(expf(bf2f(p.logdt[d])));
    const float zr = dt * are, zi = dt * aim;
    float wr, wi;
    cmul(cre, cim, dt * bre, dt * bim, wr, wi);
    const float m = wmax63(fmaxf(fabsf(wr), fabsf(wi)));
    int ex = (int)((unsigned)__builtin_amdgcn_readlane((int)f2u(m), 63) >> 23) & 255;
    ex = ex < 2 ? 2 : (ex > 250 ? 250 : ex);
    const float sc = u2f((unsigned)(253 - ex) << 23), isc = u2f((unsigned)(ex + 1) << 23);
    wr *= sc; wi *= sc;
    if (l == 0) *(LDS float*)(sm + PP_S + 4 * w) = isc;
    const float zl = zr * 1.44269504089f, rv = zi * 0.15915494309f;
    auto E = [&](float x, float& er, float& ei) {
      float mag = __builtin_amdgcn_exp2f(zl * x), ph = __builtin_amdgcn_fractf(rv * x);
      er = mag * __builtin_amdgcn_cosf(ph); ei = mag * __builtin_amdgcn_sinf(ph); };
    LDS char* Pb = sm + PP_P + ch * (41 * 528) + n * 4;
    LDS char* Qb = sm + PP_Q + ch * (16 * 528) + n * 4;
    auto stP = [&](int row, float r, float i) { *(LDS unsigned*)(Pb + row * 528) = pk_f16(r, i); };
    auto stQ = [&](int row, float r, float i) { *(LDS unsigned*)(Qb + row * 528) = pk_f16(r, -i); };
    {
      float qr[16], qi[16];
      qr[0] = 1.f; qi[0] = 0.f; E(8.f, qr[1], qi[1]);
#pragma unroll
      for (int j = 2; j < 16; ++j) cmul(qr[j / 2], qi[j / 2], qr[j - j / 2], qi[j - j / 2], qr[j], qi[j]);
#pragma unroll
      for (int j = 0; j < 16; ++j) stQ(j, qr[j], qi[j]);
    }
    float e128r, e128i, e256r, e256i, e512r, e512i, e1kr, e1ki, e2kr, e2ki;
    E(128.f, e128r, e128i); E(256.f, e256r, e256i); E(512.f, e512r, e512i); E(1024.f, e1kr, e1ki); E(2048.f, e2kr, e2ki);
    float ar[16], ai[16];
    {
      float er[8], ei[8];
      er[0] = 1.f; ei[0] = 0.f; E(1.f, er[1], ei[1]);
#pragma unroll
      for (int j = 2; j < 8; ++j) cmul(er[j / 2], ei[j / 2], er[j - j / 2], ei[j - j / 2], er[j], ei[j]);
      ar[0] = wr; ai[0] = wi;
#pragma unroll
      for (int j = 1; j < 8; ++j) cmul(wr, wi, er[j], ei[j], ar[j], ai[j]);
#pragma unroll
      for (int j = 0; j < 8; ++j) cmul(ar[j], ai[j], e128r, e128i, ar[8 + j], ai[8 + j]);
#pragma unroll
      for (int j = 0; j < 16; ++j) stP(j, ar[j], ai[j]);
    }
    float br[8], bi[8];
#pragma unroll
    for (int j = 0; j < 8; ++j) { cmul(ar[2 * (j & 3) + 8 * (j >> 2)], ai[2 * (j & 3) + 8 * (j >> 2)], e256r, e256i, br[j], bi[j]); stP(16 + j, br[j], bi[j]); }
    float cr[8], ci[8];
#pragma unroll
    for (int j = 0; j < 8; ++j) {
      const int e = j & 1, ip = j >> 1;
      float sr = ip < 2 ? ar[4 * e + 8 * ip] : br[2 * e + 4 * (ip - 2)], si = ip < 2 ? ai[4 * e + 8 * ip] : bi[2 * e + 4 * (ip - 2)];
      cmul(sr, si, e512r, e512i, cr[j], ci[j]); stP(24 + j, cr[j], ci[j]); }
#pragma unroll
    for (int j = 0; j < 8; ++j) {
      float sr = j < 2 ? ar[8 * j] : (j < 4 ? br[4 * (j - 2)] : cr[2 * (j - 4)]);
      float si = j < 2 ? ai[8 * j] : (j < 4 ? bi[4 * (j - 2)] : ci[2 * (j - 4)]);
      float r, i; cmul(sr, si, e1kr, e1ki, r, i); stP(32 + j, r, i); }
    { float r, i; cmul(wr, wi, e2kr, e2ki, r, i); stP(40, r, i); }
  } else {
    const int t = tid - 256, wv = w - 4;
    const unsigned scr = (unsigned)(size_t)sm + PP_S + 64;
    if (wv == 3 && pb < CTB) {
      unsigned long long pc; unsigned o1, o2;
      asm volatile("s_getpc_b64 %0\n.Lct%=:\n\ts_mov_b32 %1, .Lct%=-_Z2k12P1\n\ts_mov_b32 %2, _Z2k22P2-_Z2k12P1" : "=s"(pc), "=s"(o1), "=s"(o2));
#pragma unroll
      for (int i = 0; i < 4; ++i) {
        const unsigned o = (unsigned)(i * 64 + l) * 128u;
        if (o < o2 + CTK2) dma4v((const char*)(pc - o1) + o, scr);
      }
    }
    const u16* tw = ((l & 31) < 14 ? p.wg : p.wu) + (size_t)(lt * F_ + 896 * (pb & 1) + ((l & 31) < 14 ? (l & 31) : (l & 31) - 14) * 64);
    if (wv < 2) { if ((l & 31) < 28) dma4v(tw + (size_t)(32 * (TS0 + 2 * wv + (l >> 5))) * F_, scr); }
    else if (wv == 2) dma4(2u * ((512 * ((pb & 7) >> 1) + 16 * lt + (l >> 3)) * D_) + (l & 7) * 128, (const char*)p.u, scr);
    else dma4(2u * ((512 * ((pb & 7) >> 1) + 16 * lt + 8 + (l >> 3)) * D_) + (l & 7) * 128, (const char*)p.u, scr);
    const u32x4 ut0 = *(const u32x4*)(p.u + (size_t)(64 * lt + (t >> 3)) * D_ + 64 * dtile + 8 * (t & 7));
    const u32x4 ut1 = *(const u32x4*)(p.u + (size_t)(64 * lt + 32 + (t >> 3)) * D_ + 64 * dtile + 8 * (t & 7));
#pragma unroll
    for (int k = 0; k < 2; ++k) {
      const int i = wv + 4 * k, f = 7 * pb + i;
      if (i < 7) glds16(p.wd + (size_t)(16 * (f >> 4) + (l >> 2)) * D_ + 32 * (f & 15) + 8 * (l & 3), sm + PP_W + i * 1024);
    }
    const u32x4 gwv = *(const u32x4*)(p.gw + 8 * l);
    const u32x4 gx0 = *(const u32x4*)(p.u + (size_t)(8 * pb + 2 * wv) * D_ + 8 * l);
    const u32x4 gx1 = *(const u32x4*)(p.u + (size_t)(8 * pb + 2 * wv + 1) * D_ + 8 * l);
    {
      const u32x4 ones = {~0u, ~0u, ~0u, ~0u};
      st16wt(p.ws, (unsigned)WS_AT + (unsigned)(pb * 8192 + t * 16), ones);
      st16wt(p.ws, (unsigned)WS_AT + (unsigned)(pb * 8192 + 4096 + t * 16), ones);
    }
    float s0 = 0.f, s1 = 0.f;
#pragma unroll
    for (int e = 0; e < 4; ++e) {
      const float gl = bflo(gwv[e]), gh = bfhi(gwv[e]);
      unsigned p0 = pk_bf16(bflo(gx0[e]) * gl, bfhi(gx0[e]) * gh), p1 = pk_bf16(bflo(gx1[e]) * gl, bfhi(gx1[e]) * gh);
      s0 += bflo(p0); s0 += bfhi(p0); s1 += bflo(p1); s1 += bfhi(p1);
    }
    s0 = wsum63(s0); s1 = wsum63(s1);
    if (l == 63) {
      f32x2 gg = {bfr(sigmoidf_(bfr(s0))), bfr(sigmoidf_(bfr(s1)))};
      *(f32x2*)(p.ws + WS_G + (8 * pb + 2 * wv) * 4) = gg;
    }
#pragma unroll
    for (int e = 0; e < 4; ++e) {
      *(LDS unsigned*)(sm + PP_T + (t >> 3) * 132 + (t & 7) * 16 + 4 * e) = ut0[e];
      *(LDS unsigned*)(sm + PP_T + (32 + (t >> 3)) * 132 + (t & 7) * 16 + 4 * e) = ut1[e];
    }
    if (wv == 3) {
      for (int s = TS0 + 4; s < TS0 + 4 + TSPLIT; ++s) {
        if (l < 28) dma4v(tw + (size_t)(32 * s) * F_, scr);
        __builtin_amdgcn_s_sleep(TSLP);
      }
      asm volatile("s_waitcnt vmcnt(%0)" :: "n"(TSPLIT) : "memory");
    } else
    WAIT_V(0);
  }
  ST(p.ws, 224 + pb, 1);
  syncthreads();
  __builtin_amdgcn_s_setprio(PP2);
  if (w < 6) {
    const int c2 = w >= 3, T = w - 3 * c2;
    LDS char* pa = sm + PP_P + c2 * (41 * 528) + (16 * T + (l & 15)) * 528 + (l >> 4) * 16;
    LDS char* pq = sm + PP_Q + c2 * (16 * 528) + (l & 15) * 528 + (l >> 4) * 16;
    f32x4 acc0 = {0.f, 0.f, 0.f, 0.f}, acc1 = {0.f, 0.f, 0.f, 0.f};
#pragma unroll
    for (int s = 0; s < 4; ++s) {
      acc0 = __builtin_amdgcn_mfma_f32_16x16x32_f16(*(LDS f16x8*)(pa + s * 64), *(LDS f16x8*)(pq + s * 64), acc0, 0, 0, 0);
      acc1 = __builtin_amdgcn_mfma_f32_16x16x32_f16(*(LDS f16x8*)(pa + 256 + s * 64), *(LDS f16x8*)(pq + 256 + s * 64), acc1, 0, 0, 0);
    }
    const float is0 = *(LDS float*)(sm + PP_S + 8 * c2), is1 = *(LDS float*)(sm + PP_S + 8 * c2 + 4);
    LDS float* kc = (LDS float*)(sm + PP_KC) + c2 * 656;
    const int j = l & 15;
#pragma unroll
    for (int r = 0; r < 4; ++r) {
      const int row = 4 * (l >> 4) + r;
      int ci_;
      if (T == 0) ci_ = (row & 7) + 128 * (row >> 3) + 8 * j;
      else if (T == 1) ci_ = row < 8 ? 256 + (row & 3) + 64 * (row >> 2) + 4 * j : 384 + (row & 1) + 32 * ((row - 8) >> 1) + 2 * j;
      else ci_ = row < 8 ? 512 + 16 * row + j : ((row == 8 && j == 0) ? 640 : 641);
      kc[ci_] = acc0[r] * is0 + acc1[r] * is1;
    }
  }
  if (w >= 4) {
    const int t = tid - 256;
    if (w == TW2) {
      const unsigned scr = (unsigned)(size_t)sm + PP_S + 64;
      const u16* tw = ((l & 31) < 14 ? p.wg : p.wu) + (size_t)(lt * F_ + 896 * (pb & 1) + ((l & 31) < 14 ? (l & 31) : (l & 31) - 14) * 64);
      for (int s = TS0 + 4 + TSPLIT + (l >> 5); s < 16; s += 2) {
        if ((l & 31) < 28) dma4v(tw + (size_t)(32 * s) * F_, scr);
        __builtin_amdgcn_s_sleep(TSLP2);
      }
    }
    if (w >= 6) {
      const int h = l >> 5, blk = (l >> 4) & 1, q = (l & 15) >> 2, pp = l & 3;
#pragma unroll
      for (int k = 0; k < 4; ++k) {
        const int i = 4 * (w - 6) + k, f = 7 * pb + i;
        if (i < 7) {
          LDS char* b0 = sm + PP_W + i * 1024 + (4 * h + q) * 64 + (16 * blk + 4 * pp) * 2;
          s16x4 a = trread(b0), b = trread(b0 + 8 * 64);
          s16x8 r = __builtin_shufflevector(a, b, 0, 1, 2, 3, 4, 5, 6, 7);
          st16wt(p.ws, (unsigned)WS_WD + (unsigned)(((f & 15) * KS2 + (f >> 4)) * 1024 + l * 16), __builtin_bit_cast(u32x4, r));
        }
      }
    }
#pragma unroll
    for (int k = 0; k < 2; ++k) {
      const int dd = (t >> 3) + 32 * k, lc = (t & 7) * 8;
      u32x4 o4;
#pragma unroll
      for (int e = 0; e < 4; ++e) {
        u16 a = *(LDS u16*)(sm + PP_T + (lc + 2 * e) * 132 + 2 * dd), b = *(LDS u16*)(sm + PP_T + (lc + 2 * e + 1) * 132 + 2 * dd);
        o4[e] = (unsigned)a | ((unsigned)b << 16);
      }
      st16wt(p.ws, (unsigned)WS_UT + (unsigned)(((64 * dtile + dd) * L_ + 64 * lt + lc) * 2), o4);
    }
  }
  syncthreads();
  ST(p.ws, 224 + pb, 2);
  {
    const int c2 = w >> 2, c = tid & 255, k = 255 - c, cls = w & 3;
    LDS float* kc = (LDS float*)(sm + PP_KC) + c2 * 656;
    u32x4 ov;
    if (cls < 2) {
      const float lo = kc[384 + k], hi = kc[385 + k];
      const unsigned hh = pk_bf16(hi, hi), ll = pk_bf16(lo, lo);
      ov[0] = hh; ov[1] = (k & 1) ? hh : ((hh & 0xffffu) | (ll & 0xffff0000u)); ov[2] = ll; ov[3] = ll;
    } else if (cls == 2) {
      const float x0 = kc[256 + 2 * k], x1 = kc[257 + 2 * k], x2 = kc[258 + 2 * k];
      ov[0] = pk_bf16(x2, x2); ov[1] = pk_bf16(x1, x1); ov[2] = pk_bf16(x1, x0); ov[3] = pk_bf16(x0, x0);
    } else {
      const bool c_ = l < 32;
      LDS float* b = kc + (c_ ? 128 + 4 * k : 8 * k);
      const float v0 = b[c_ ? 4 : 7], v1 = b[c_ ? 3 : 6], v2 = b[c_ ? 2 : 5], v3 = b[c_ ? 2 : 4];
      const float v4 = b[c_ ? 2 : 3], v5 = b[c_ ? 1 : 2], v6 = b[c_ ? 0 : 1], v7 = b[0];
      ov[0] = pk_bf16(v0, v1); ov[1] = pk_bf16(v2, v3); ov[2] = pk_bf16(v4, v5); ov[3] = pk_bf16(v6, v7);
    }
    st16wt(p.ws, (unsigned)WS_KR + (unsigned)(((2 * pb + c2) * KROW + 8 * c) * 2), ov);
    if (tid < 8) { u32x4 z = {0u, 0u, 0u, 0u}; st16wt(p.ws, (unsigned)WS_KR + (unsigned)(((2 * pb + (tid >> 2)) * KROW + 2048 + 8 * (tid & 3)) * 2), z); }
  }
  WAIT_V(0);
  ST(p.ws, 224 + pb, 3);
}

__global__ void __launch_bounds__(512, 4) k1(P1 p) {
  extern __shared__ __attribute__((aligned(16))) char smem[];
  LDS char* sm = (LDS char*)smem;
  if (blockIdx.x < 224) gemm1(p, sm, blockIdx.x); else prep(p, sm, blockIdx.x - 224);
}

struct P2 { const u16* dskip; u16* out; char* ws; };
constexpr int CV_K = 0;
constexpr int CV_U = 2 * 4 * 4160;
constexpr int CV_R = CV_U + 2 * 4608;
constexpr int K2_LDS = 65536 + 8192;

__device__ __forceinline__ void conv(const P2& p, LDS char* sm, int cb) {
  const int tid = threadIdx.x, w = __builtin_amdgcn_readfirstlane(tid >> 6), l = tid & 63;
  ST(p.ws, 512 + cb, 0);
  __builtin_amdgcn_s_setprio(3);
  const u16* ut = (const u16*)(p.ws + WS_UT) + (size_t)(2 * cb) * L_;
  const u16* kr = (const u16*)(p.ws + WS_KR) + (size_t)(2 * cb) * KROW;
  u32x4 uv[2];
#pragma unroll
  for (int c = 0; c < 2; ++c) uv[c] = *(const u32x4*)(ut + c * L_ + 8 * tid);
  u32x4 k0[3]; u32x2 k1v[3];
#pragma unroll
  for (int i = 0; i < 3; ++i) {
    int job = tid + 256 * i; job = job < 520 ? job : 519;
    const int c2 = job >= 260, c = job - 260 * c2;
    k0[i] = *(const u32x4*)(kr + c2 * KROW + 8 * c);
    k1v[i] = *(const u32x2*)(kr + c2 * KROW + 8 * c + 8);
    if (c == 259) { k1v[i][0] = 0u; k1v[i][1] = 0u; }
  }
  const float dsk = bf2f(p.dskip[2 * cb + (w >> 1)]);
#pragma unroll
  for (int c = 0; c < 2; ++c) {
    *(LDS u32x4*)(sm + CV_U + c * 4608 + 512 + 16 * tid) = uv[c];
    if (tid < 32) { u32x4 z = {0u, 0u, 0u, 0u}; *(LDS u32x4*)(sm + CV_U + c * 4608 + 16 * tid) = z; }
  }
#pragma unroll
  for (int i = 0; i < 3; ++i) {
    const int job = tid + 256 * i;
    if (job < 520) {
      const int c2 = job >= 260, c = job - 260 * c2;
      unsigned dd[6] = {k0[i][0], k0[i][1], k0[i][2], k0[i][3], k1v[i][0], k1v[i][1]};
      LDS char* b = sm + CV_K + c2 * (4 * 4160) + 16 * c;
      u32x4 c0 = {dd[0], dd[1], dd[2], dd[3]}, c2v = {dd[1], dd[2], dd[3], dd[4]}, c1, c3;
#pragma unroll
      for (int e = 0; e < 4; ++e) { c1[e] = __builtin_amdgcn_alignbit(dd[e + 1], dd[e], 16); c3[e] = __builtin_amdgcn_alignbit(dd[e + 2], dd[e + 1], 16); }
      *(LDS u32x4*)(b) = c0; *(LDS u32x4*)(b + 4160) = c1; *(LDS u32x4*)(b + 2 * 4160) = c2v; *(LDS u32x4*)(b + 3 * 4160) = c3;
    }
  }
  syncthreads();
  ST(p.ws, 512 + cb, 1);
  const int ch = w >> 1, rh = w & 1, j = l & 15, g = l >> 4;
  const int e = 3 - (j & 3);
  LDS char* ka = sm + CV_K + ch * (4 * 4160) + e * 4160 + 2 * (2031 - j + 8 * g - e);
  LDS char* ub = sm + CV_U + ch * 4608 + 2 * (240 + 16 * j + 8 * g);
  f32x4 acc[8];
#pragma unroll
  for (int c = 0; c < 8; ++c) acc[c] = f32x4{0.f, 0.f, 0.f, 0.f};
#pragma unroll
  for (int rr = 0; rr < 4; ++rr) {
    const int r = 4 * rh + rr;
    bf16x8 bf[8];
#pragma unroll
    for (int t = 0; t < 8; ++t) bf[t] = *(LDS bf16x8*)(ub + 512 * t - 64 * CVR);
#pragma unroll
    for (int q = 0; q < 8; ++q) {
      LDS char* a = ka - 64 * (8 * CVQ + CVR);
      s16x4 lo = *(LDS s16x4*)a, hi = *(LDS s16x4*)(a + 8);
      bf16x8 af = cat8(lo, hi);
#pragma unroll
      for (int c = q; c < 8; ++c) acc[c] = __builtin_amdgcn_mfma_f32_16x16x32_bf16(af, bf[c - q], acc[c], 0, 0, 0);
    }
  }
  ST(p.ws, 512 + cb, 2);
  LDS char* rb = sm + CV_R;
#pragma unroll
  for (int c = 0; c < 4; ++c) *(LDS f32x4*)(rb + (w * 4 + c) * 1024 + l * 16) = acc[4 * (rh ^ 1) + c];
  syncthreads();
  u16* at = (u16*)(p.ws + WS_AT) + (size_t)(2 * cb + ch) * L_;
#pragma unroll
  for (int c = 0; c < 4; ++c) {
    f32x4 o = *(LDS f32x4*)(rb + ((w ^ 1) * 4 + c) * 1024 + l * 16);
    f32x4 y = acc[4 * rh + c] + o;
    const int lpos = 256 * (4 * rh + c) + 16 * j + 4 * g;
    u32x2 uu = *(LDS u32x2*)(sm + CV_U + ch * 4608 + 512 + 2 * lpos);
    unsigned y0 = pk_bf16(y[0], y[1]), y1 = pk_bf16(y[2], y[3]);
    unsigned s0 = pk_bf16(bflo(uu[0]) * dsk, bfhi(uu[0]) * dsk), s1 = pk_bf16(bflo(uu[1]) * dsk, bfhi(uu[1]) * dsk);
    unsigned a0 = pk_bf16(bflo(y0) + bflo(s0), bfhi(y0) + bfhi(s0)), a1 = pk_bf16(bflo(y1) + bflo(s1), bfhi(y1) + bfhi(s1));
    unsigned long long v = (unsigned long long)a0 | ((unsigned long long)a1 << 32);
    if (v == ~0ull) v = 0x7FC07FC07FC07FC0ull;
    __hip_atomic_store((GLB unsigned long long*)(at + lpos), v, __ATOMIC_RELAXED, __HIP_MEMORY_SCOPE_AGENT);
  }
  ST(p.ws, 512 + cb, 3);
}

#define PFX 9
constexpr int PF = PFX;
#define APRE 4
struct F2 { bf16x8 a0, a1, b0, b1; };
#define GLD16(dst, voff, sbase, off) asm volatile("global_load_dwordx4 %0, %1, %2 offset:%3" : "=v"(dst) : "v"(voff), "s"(sbase), "i"(off))
template <int S> __device__ __forceinline__ void g2_ld(F2& f, unsigned vo, const char* A0, const char* B0) {
  const char *a = A0 + (S >> 2) * 4096, *b = B0 + (S >> 2) * 4096;
  GLD16(f.a0, vo, a, (S & 3) * 1024); GLD16(f.b0, vo, b, (S & 3) * 1024);
  GLD16(f.a1, vo, a + KS2 * 1024, (S & 3) * 1024); GLD16(f.b1, vo, b + KS2 * 1024, (S & 3) * 1024); }
template <int S> __device__ __forceinline__ void g2_pro(F2 (&f)[PF], unsigned vo, const char* A0, const char* B0) {
  g2_ld<S>(f[S], vo, A0, B0);
  if constexpr (S + 1 < PF) g2_pro<S + 1>(f, vo, A0, B0); }
template <int S> __device__ __forceinline__ void g2_step(F2 (&f)[PF], unsigned vo, const char* A0, const char* B0, f32x16 (&acc)[2][2],
                                                         unsigned va, const char* AT, unsigned long long (&av8)[4]) {
  F2& c = f[S % PF];
  constexpr int pend = (S + PF <= 28 ? PF : 28 - S) - 1;
  asm volatile("s_waitcnt vmcnt(%4)" : "+v"(c.a0), "+v"(c.a1), "+v"(c.b0), "+v"(c.b1) : "i"(4 * pend + (S + PF > 27 + APRE ? 4 : 0)) : "memory");
  acc[0][0] = MFMA32(c.a0, c.b0, acc[0][0]); acc[0][1] = MFMA32(c.a0, c.b1, acc[0][1]);
  acc[1][0] = MFMA32(c.a1, c.b0, acc[1][0]); acc[1][1] = MFMA32(c.a1, c.b1, acc[1][1]);
  SCHED();
  if constexpr (S + PF < 28) g2_ld<S + PF>(c, vo, A0, B0);
  if constexpr (S + PF == 27 + APRE) {
#pragma unroll
    for (int i = 0; i < 4; ++i)
      asm volatile("global_load_dwordx2 %0, %1, %2 sc1" : "=v"(av8[i]) : "v"(va), "s"(AT + (size_t)i * 16 * L_ * 2) : "memory");
  }
  SCHED();
  if constexpr (S + 1 < 28) g2_step<S + 1>(f, vo, A0, B0, acc, va, AT, av8);
}
__device__ __forceinline__ void gemm2(const P2& p, LDS char* sm, int b) {
  const int tid = threadIdx.x, w = __builtin_amdgcn_readfirstlane(tid >> 6), l = tid & 63;
  const int xcd = b & 7, idx = b >> 3;
  const int mt = 4 * xcd + (idx & 3), nt = idx >> 2;
#define K2NT nt
#define K2MT mt
  const char* A0 = p.ws + WS_WD + ((size_t)(2 * (K2NT)) * KS2 + 28 * w) * 1024;
  const char* B0 = p.ws + WS_H + ((size_t)(2 * (K2MT)) * KS2 + 28 * w) * 1024;
  f32x16 acc[2][2];
#pragma unroll
  for (int i = 0; i < 2; ++i)
#pragma unroll
    for (int j = 0; j < 2; ++j)
#pragma unroll
      for (int r = 0; r < 16; ++r) acc[i][j][r] = 0.f;
  ST(p.ws, 768 + b, 0);
  __builtin_amdgcn_s_setprio(G2P);
  const int tl = 16 * w + (l & 15), tok = 64 * mt + tl;
  float g;
  asm volatile("global_load_dword %0, %1, %2" : "=v"(g) : "v"(tok * 4), "s"(p.ws + WS_G));
  F2 f[PF];
  g2_pro<0>(f, l * 16, A0, B0);
  SCHED();
  unsigned long long av8[4];
  g2_step<0>(f, l * 16, A0, B0, acc, (unsigned)(((tid >> 4) * L_ + 4 * (tid & 15)) * 2),
             p.ws + WS_AT + ((size_t)(64 * nt) * L_ + 64 * mt) * 2, av8);
  asm volatile("s_waitcnt vmcnt(0)" : "+v"(g), "+v"(av8[0]), "+v"(av8[1]), "+v"(av8[2]), "+v"(av8[3]) :: "memory");
  ST(p.ws, 768 + b, 1);
  GLB unsigned long long* at = (GLB unsigned long long*)((const u16*)(p.ws + WS_AT) + (size_t)(64 * nt + (tid >> 4)) * L_ + 64 * mt + 4 * (tid & 15));
#pragma unroll
  for (int i = 0; i < 2; ++i)
#pragma unroll
    for (int j = 0; j < 2; ++j)
#pragma unroll
      for (int q = 0; q < 4; ++q) {
        f32x4 v = {acc[i][j][4 * q], acc[i][j][4 * q + 1], acc[i][j][4 * q + 2], acc[i][j][4 * q + 3]};
        *(LDS f32x4*)(sm + w * 16384 + ((2 * i + j) * 4 + q) * 1024 + l * 16) = v;
      }
  for (unsigned spins = 0; spins < (1u << 22); ++spins) {
    const bool bad = av8[0] == ~0ull || av8[1] == ~0ull || av8[2] == ~0ull || av8[3] == ~0ull;
    if (__builtin_amdgcn_ballot_w64(bad) == 0ull) break;
    __builtin_amdgcn_s_sleep(1);
#pragma unroll
    for (int i = 0; i < 4; ++i) av8[i] = __hip_atomic_load(at + (size_t)i * 16 * L_ / 4, __ATOMIC_RELAXED, __HIP_MEMORY_SCOPE_AGENT);
  }
#pragma unroll
  for (int i = 0; i < 4; ++i) *(LDS unsigned long long*)(sm + 65536 + ((tid >> 4) + 16 * i) * 128 + (tid & 15) * 8) = av8[i];
  syncthreads();
  ST(p.ws, 768 + b, 2);
  const float og = bfr(1.f - g);
  const int q = l >> 4, qq = (l & 15) >> 2, pp = l & 3;
#pragma unroll
  for (int k = 0; k < 2; ++k) {
    unsigned ov[4];
#pragma unroll
    for (int h = 0; h < 2; ++h) {
      LDS char* rp = sm + ((2 * k + (w >> 1)) * 4 + q) * 1024 + (16 * (w & 1) + (l & 15) + 32 * h) * 16;
      f32x4 s = *(LDS f32x4*)rp;
#pragma unroll
      for (int ww = 1; ww < 4; ++ww) s += *(LDS f32x4*)(rp + ww * 16384);
      s16x4 av = trread(sm + 65536 + (32 * k + 8 * q + 4 * h + qq) * 128 + (16 * w + 4 * pp) * 2);
      float o[4];
#pragma unroll
      for (int e = 0; e < 4; ++e) o[e] = bfr(g * bf2f((u16)av[e])) + bfr(og * bfr(s[e]));
      ov[2 * h] = pk_bf16(o[0], o[1]); ov[2 * h + 1] = pk_bf16(o[2], o[3]);
    }
    u32x4 o4 = {ov[0], ov[1], ov[2], ov[3]};
    *(u32x4*)(p.out + (size_t)tok * D_ + 64 * nt + 32 * k + 8 * q) = o4;
  }
  ST(p.ws, 768 + b, 3);
}

__global__ void __launch_bounds__(256, 2) k2(P2 p) {
  extern __shared__ __attribute__((aligned(16))) char smem[];
  LDS char* sm = (LDS char*)smem;
  if (blockIdx.x < 256) conv(p, sm, blockIdx.x); else gemm2(p, sm, blockIdx.x - 256);
}

#if defined(__HIP_DEVICE_COMPILE__)
#pragma clang attribute pop
#endif
extern "C" void kernel_launch(void* const* d_in, const int* in_sizes, int n_in, void* d_out, int out_size,
                              void* d_ws, size_t ws_size, hipStream_t stream) {
  P1 a;
  a.u = (const u16*)d_in[0]; a.are = (const u16*)d_in[1]; a.aim = (const u16*)d_in[2]; a.bre = (const u16*)d_in[3];
  a.bim = (const u16*)d_in[4]; a.cre = (const u16*)d_in[5]; a.cim = (const u16*)d_in[6]; a.logdt = (const u16*)d_in[7];
  a.wg = (const u16*)d_in[10]; a.wu = (const u16*)d_in[11]; a.wd = (const u16*)d_in[12];
  a.gw = (const u16*)d_in[13]; a.ws = (char*)d_ws;
  P2 b; b.dskip = (const u16*)d_in[8]; b.out = (u16*)d_out; b.ws = (char*)d_ws;
  (void)hipFuncSetAttribute((const void*)k1, hipFuncAttributeMaxDynamicSharedMemorySize, 81920);
  (void)hipFuncSetAttribute((const void*)k2, hipFuncAttributeMaxDynamicSharedMemorySize, K2_LDS);
  hipLaunchKernelGGL(k1, dim3(480), dim3(512), 81920, stream, a);
  hipLaunchKernelGGL(k2, dim3(512), dim3(256), K2_LDS, stream, b);
}
```

```cpp
#include <hip/hip_runtime.h>
#include <hip/hip_bf16.h>
#include <stdint.h>

#if defined(__HIP_DEVICE_COMPILE__)
#pragma clang attribute push(__attribute__((target("no-packed-fp32-ops"))), apply_to = function)
#endif
typedef unsigned short u16;
typedef __attribute__((ext_vector_type(4))) short s16x4;
typedef __attribute__((ext_vector_type(8))) short s16x8;
typedef __attribute__((ext_vector_type(8))) __bf16 bf16x8;
typedef __attribute__((ext_vector_type(2))) __bf16 bf16x2;
typedef __attribute__((ext_vector_type(8))) _Float16 f16x8;
typedef __attribute__((ext_vector_type(2))) _Float16 f16x2;
typedef __attribute__((ext_vector_type(16))) float f32x16;
typedef __attribute__((ext_vector_type(4))) float f32x4;
typedef __attribute__((ext_vector_type(2))) float f32x2;
typedef __attribute__((ext_vector_type(4))) unsigned u32x4;
typedef __attribute__((ext_vector_type(2))) unsigned u32x2;
#define LDS __attribute__((address_space(3)))
#define GLB __attribute__((address_space(1)))

constexpr int L_ = 2048, D_ = 512, N_ = 128, F_ = 1792;
constexpr int KS2 = F_ / 16;
constexpr int KROW = 2080;
constexpr size_t WS_H    = 0;
constexpr size_t WS_WD   = WS_H + (size_t)64 * KS2 * 1024;
constexpr size_t WS_UT   = WS_WD + (size_t)16 * KS2 * 1024;
constexpr size_t WS_KR   = WS_UT + (size_t)D_ * L_ * 2;
constexpr size_t WS_AT   = WS_KR + (size_t)D_ * KROW * 2 + 4096;
constexpr size_t WS_G    = WS_AT + (size_t)D_ * L_ * 2;
constexpr unsigned WS_BYTES = (unsigned)(WS_G + (size_t)L_ * 4);

__device__ __forceinline__ float u2f(unsigned v) { return __builtin_bit_cast(float, v); }
__device__ __forceinline__ unsigned f2u(float v) { return __builtin_bit_cast(unsigned, v); }
__device__ __forceinline__ void syncthreads() {
  __builtin_amdgcn_fence(__ATOMIC_RELEASE, "workgroup"); __builtin_amdgcn_s_barrier(); __builtin_amdgcn_fence(__ATOMIC_ACQUIRE, "workgroup"); }
__device__ __forceinline__ float shx(float v, int lane, int m) {
  return u2f((unsigned)__builtin_amdgcn_ds_bpermute((lane ^ m) << 2, (int)f2u(v))); }
__device__ __forceinline__ float bf2f(u16 v) { return u2f((unsigned)v << 16); }
__device__ __forceinline__ unsigned pk_bf16(float a, float b) {
  f32x2 t = {a, b}; bf16x2 r = __builtin_convertvector(t, bf16x2); return __builtin_bit_cast(unsigned, r); }
__device__ __forceinline__ float bflo(unsigned p) { return u2f(p << 16); }
__device__ __forceinline__ float bfhi(unsigned p) { return u2f(p & 0xffff0000u); }
__device__ __forceinline__ float bfr(float a) { return bflo(pk_bf16(a, 0.f)); }
__device__ __forceinline__ unsigned pk_f16(float a, float b) {
  f32x2 t = {a, b}; f16x2 r = __builtin_convertvector(t, f16x2); return __builtin_bit_cast(unsigned, r); }
__device__ __forceinline__ float sigmoidf_(float x) { return __builtin_amdgcn_rcpf(1.f + __builtin_amdgcn_exp2f(-1.44269504089f * x)); }
#define WAIT_V(n) asm volatile("s_waitcnt vmcnt(" #n ")" ::: "memory")
#define WAIT_L(n) asm volatile("s_waitcnt lgkmcnt(" #n ")" ::: "memory")
#define BAR() __builtin_amdgcn_s_barrier()
#define SCHED() __builtin_amdgcn_sched_barrier(0)
#define ST(ws, row, i)
__device__ __forceinline__ void glds16(const void* g, LDS char* l) {
  __builtin_amdgcn_global_load_lds((const GLB void*)g, (LDS void*)l, 16, 0, 0); }

__device__ __forceinline__ void st16wt(char* ws, unsigned off, u32x4 v) {
  __builtin_amdgcn_raw_buffer_store_b128(v, __builtin_amdgcn_make_buffer_rsrc(ws, 0, WS_BYTES, 0x00020000), off, 0, 16); }
__device__ __forceinline__ s16x4 trread(LDS char* p) { return __builtin_amdgcn_ds_read_tr16_b64_v4i16((LDS s16x4*)p); }
__device__ __forceinline__ bf16x8 cat8(s16x4 a, s16x4 b) {
  s16x8 r = __builtin_shufflevector(a, b, 0, 1, 2, 3, 4, 5, 6, 7); return __builtin_bit_cast(bf16x8, r); }

struct P1 {
  const u16 *u, *are, *aim, *bre, *bim, *cre, *cim, *logdt, *wg, *wu, *wd, *gw;
  char* ws;
};

#define DSR_TR(dst, addr, off) asm volatile("ds_read_b64_tr_b16 %0, %1 offset:%2" : "=v"(dst) : "v"(addr), "i"(off))
#define DSR_128(dst, addr, off) asm volatile("ds_read_b128 %0, %1 offset:%2" : "=v"(dst) : "v"(addr), "i"(off))
__device__ __forceinline__ void dma16(unsigned voff, const char* sbase, unsigned ldsb) {
  asm volatile("s_mov_b32 m0, %2\n\ts_nop 0\n\tglobal_load_lds_dwordx4 %0, %1" :: "v"(voff), "s"(sbase), "s"(ldsb) : "memory"); }
__device__ __forceinline__ void dma4(unsigned voff, const char* sbase, unsigned ldsb) {
  asm volatile("s_mov_b32 m0, %2\n\ts_nop 0\n\tglobal_load_lds_dword %0, %1" :: "v"(voff), "s"(sbase), "s"(ldsb) : "memory"); }
struct Frag { s16x4 a0l, a0h, a1l, a1h; bf16x8 b0, b1; };
#define FRAG_WAIT(f) asm volatile("s_waitcnt lgkmcnt(0)" : "+v"(f.a0l), "+v"(f.a0h), "+v"(f.a1l), "+v"(f.a1h), "+v"(f.b0), "+v"(f.b1) :: "memory")
#define MFMA32(a, b, c) __builtin_amdgcn_mfma_f32_32x32x16_bf16(a, b, c, 0, 0, 0)
struct G1 {
  unsigned a0, a1, bx[4];
  unsigned vg, vu, vx0, vx1;
  const char *wg, *wu, *x;
  unsigned lw, lx;
};
template <int S, int KS> __device__ __forceinline__ void g1_rd0(const G1& g, Frag& f) {
  constexpr int wo = (S % 3) * 16384 + KS * 4096, xo = ((S >> 1) & 1) * 16384;
  DSR_TR(f.a0l, g.a0, wo); DSR_TR(f.a0h, g.a0, wo + 1024); DSR_128(f.b0, g.bx[2 * (S & 1) + KS], xo); }
template <int S, int KS> __device__ __forceinline__ void g1_rd1(const G1& g, Frag& f) {
  constexpr int wo = (S % 3) * 16384 + KS * 4096, xo = ((S >> 1) & 1) * 16384;
  DSR_128(f.b1, g.bx[2 * (S & 1) + KS], xo + 4096); DSR_TR(f.a1l, g.a1, wo); DSR_TR(f.a1h, g.a1, wo + 1024); }
template <int S> __device__ __forceinline__ void g1_issueW(const G1& g) {
  dma16(g.vg, g.wg + (size_t)S * 32 * F_ * 2, g.lw + (S % 3) * 16384);
  dma16(g.vu, g.wu + (size_t)S * 32 * F_ * 2, g.lw + (S % 3) * 16384 + 8192); }
template <int J> __device__ __forceinline__ void g1_issueX(const G1& g) {
  dma16(g.vx0, g.x + J * 128, g.lx + (J & 1) * 16384);
  dma16(g.vx1, g.x + J * 128, g.lx + (J & 1) * 16384 + 1024); }
template <int S> __device__ __forceinline__ void g1_stage(const G1& g, Frag& f0, Frag& f1, f32x16 (&acc)[2][2]) {
  FRAG_WAIT(f0);
  {
    const bf16x8 a0 = cat8(f0.a0l, f0.a0h), a1 = cat8(f0.a1l, f0.a1h);
    acc[0][0] = MFMA32(a0, f0.b0, acc[0][0]); SCHED();
    g1_rd0<S, 1>(g, f1); SCHED();
    acc[0][1] = MFMA32(a0, f0.b1, acc[0][1]); SCHED();
    g1_rd1<S, 1>(g, f1); SCHED();
    acc[1][0] = MFMA32(a1, f0.b0, acc[1][0]); SCHED();
    acc[1][1] = MFMA32(a1, f0.b1, acc[1][1]); SCHED();
  }
  FRAG_WAIT(f1);
  if constexpr (S < 15) {
    if constexpr (S >= 14) { WAIT_V(0); } else if constexpr ((S & 1) == 0) { WAIT_V(4); } else { WAIT_V(2); }
    BAR();
  }
  {
    const bf16x8 a0 = cat8(f1.a0l, f1.a0h), a1 = cat8(f1.a1l, f1.a1h);
    acc[0][0] = MFMA32(a0, f1.b0, acc[0][0]); SCHED();
    if constexpr (S < 15) g1_rd0<S + 1, 0>(g, f0);
    SCHED();
    acc[0][1] = MFMA32(a0, f1.b1, acc[0][1]); SCHED();
    if constexpr (S < 15) g1_rd1<S + 1, 0>(g, f0);
    SCHED();
    acc[1][0] = MFMA32(a1, f1.b0, acc[1][0]); SCHED();
    if constexpr (S + 3 < 16) g1_issueW<S + 3>(g);
    SCHED();
    acc[1][1] = MFMA32(a1, f1.b1, acc[1][1]); SCHED();
    if constexpr ((S & 1) == 1 && (S >> 1) + 2 < 8) g1_issueX<(S >> 1) + 2>(g);
    SCHED();
  }
  if constexpr (S < 15) g1_stage<S + 1>(g, f0, f1, acc);
}
__device__ __forceinline__ void gemm1(const P1& p, LDS char* sm, int b) {
  const int tid = threadIdx.x, w = __builtin_amdgcn_readfirstlane(tid >> 6), l = tid & 63;
  const int xcd = b & 7, idx = b >> 3;
  const int mt = 4 * (xcd >> 1) + (idx & 3), nt = 7 * (xcd & 1) + (idx >> 2);
  const int wm = w & 1, wn = w >> 1;
  const unsigned smb = (unsigned)(size_t)sm;
  G1 g;
  {
    const int krow = 4 * w + (l >> 4);
    g.vg = 2u * (krow * F_ + 8 * ((l & 15) ^ (2 * (l >> 4))));
    g.vu = 2u * (krow * F_ + 8 * ((l & 15) ^ (2 * ((l >> 4) | 4))));
    const int xr0 = 16 * w + (l >> 3);
    g.vx0 = 2u * (xr0 * D_ + 8 * ((l & 7) ^ ((xr0 >> 1) & 7)));
    g.vx1 = 2u * ((xr0 + 8) * D_ + 8 * ((l & 7) ^ (((xr0 + 8) >> 1) & 7)));
    g.wg = (const char*)(p.wg + nt * 128); g.wu = (const char*)(p.wu + nt * 128); g.x = (const char*)(p.u + (size_t)mt * 128 * D_);
    g.lw = smb + w * 1024; g.lx = smb + 49152 + w * 2048;
    const int h = l >> 5, blk = (l >> 4) & 1, q = (l & 15) >> 2, pp = l & 3, img = pp >> 1;
    g.a0 = smb + img * 8192 + (8 * h + q) * 256 + 32 * ((2 * wn) ^ (q | (img << 2))) + 16 * blk + 8 * (pp & 1);
    g.a1 = g.a0 ^ 32;
    const int xrow = 64 * wm + (l & 31);
    const unsigned bb = 49152 + xrow * 128 + 16 * (h ^ ((xrow >> 1) & 7));
#pragma unroll
    for (int c = 0; c < 4; ++c) g.bx[c] = smb + (bb ^ (c * 32));
  }
  f32x16 acc[2][2];
#pragma unroll
  for (int i = 0; i < 2; ++i)
#pragma unroll
    for (int j = 0; j < 2; ++j)
#pragma unroll
      for (int r = 0; r < 16; ++r) acc[i][j][r] = 0.f;
  Frag f0, f1;
  ST(p.ws, b, 0);
#define G1PRIO 2
  __builtin_amdgcn_s_setprio(G1PRIO);
  g1_issueX<0>(g); g1_issueW<0>(g); g1_issueW<1>(g); g1_issueX<1>(g); g1_issueW<2>(g);
  WAIT_V(6); BAR();
  ST(p.ws, b, 1);
  g1_rd0<0, 0>(g, f0); g1_rd1<0, 0>(g, f0);
  g1_stage<0>(g, f0, f1, acc);
  ST(p.ws, b, 2);
#pragma unroll
  for (int i = 0; i < 2; ++i)
#pragma unroll
    for (int j = 0; j < 2; ++j) {
      unsigned o[4];
#pragma unroll
      for (int hh = 0; hh < 2; ++hh) {
        float hv[4];
#pragma unroll
        for (int e = 0; e < 4; e += 2) {
          unsigned gp = pk_bf16(acc[i][j][8 * hh + e], acc[i][j][8 * hh + e + 1]);
          unsigned up = pk_bf16(acc[i][j][8 * hh + 4 + e], acc[i][j][8 * hh + 4 + e + 1]);
          float g0 = bflo(gp), g1 = bfhi(gp);
          unsigned sp = pk_bf16(g0 * sigmoidf_(g0), g1 * sigmoidf_(g1));
          hv[e] = bflo(sp) * bflo(up); hv[e + 1] = bfhi(sp) * bfhi(up);
        }
        o[2 * hh] = pk_bf16(hv[0], hv[1]); o[2 * hh + 1] = pk_bf16(hv[2], hv[3]);
      }
      const int tb = 4 * mt + 2 * wm + j, ks = 8 * nt + 2 * wn + i;
      u32x4 ov = {o[0], o[1], o[2], o[3]};
      st16wt(p.ws, (unsigned)WS_H + (unsigned)((tb * KS2 + ks) * 1024 + l * 16), ov);
    }
  ST(p.ws, b, 3);
}

constexpr int PP_P = 0;
constexpr int PP_Q = PP_P + 2 * 41 * 528;
constexpr int PP_KC = PP_Q + 2 * 16 * 528;
constexpr int PP_T = PP_KC + 2 * 656 * 4;
constexpr int PP_W = PP_T + 64 * 132;
constexpr int PP_S = PP_W + 7 * 1024;
static_assert(PP_S + 64 + 256 <= 81920, "lds");
static_assert(PP_W % 16 == 0 && PP_KC % 16 == 0 && PP_T % 16 == 0, "al");

__device__ __forceinline__ void cmul(float ar, float ai, float br, float bi, float& r, float& i) {
  r = ar * br - ai * bi; i = ar * bi + ai * br; }

#define TS0 3
#define TSLP 9
#define TSPLIT 3
#define TSLP2 4
#define PP1 3
#define PP2 3
#define CVP 3
#define G2P 0
#define CVR r
#define CVQ q
#define CVM 0
#define CT 1
#define CTB 256
#define CTK2 9216u
#define OWT 16
#define KDPP 1
#define TW2 7
template <int CTRL, int RM> __device__ __forceinline__ float dppf(float v) {
  return u2f((unsigned)__builtin_amdgcn_update_dpp(0, (int)f2u(v), CTRL, RM, 0xf, true)); }
__device__ __forceinline__ float wsum63(float v) {
  v += dppf<0x111, 0xf>(v); v += dppf<0x112, 0xf>(v); v += dppf<0x114, 0xf>(v); v += dppf<0x118, 0xf>(v);
  v += dppf<0x142, 0xa>(v); v += dppf<0x143, 0xc>(v); return v; }
__device__ __forceinline__ float wmax63(float v) {
  v = fmaxf(v, dppf<0x111, 0xf>(v)); v = fmaxf(v, dppf<0x112, 0xf>(v)); v = fmaxf(v, dppf<0x114, 0xf>(v)); v = fmaxf(v, dppf<0x118, 0xf>(v));
  v = fmaxf(v, dppf<0x142, 0xa>(v)); v = fmaxf(v, dppf<0x143, 0xc>(v)); return v; }
__device__ __forceinline__ void dma4v(const void* g, unsigned ldsb) {
  asm volatile("s_mov_b32 m0, %1\n\ts_nop 0\n\tglobal_load_lds_dword %0, off" :: "v"(g), "s"(ldsb) : "memory"); }

__device__ __forceinline__ void prep(const P1& p, LDS char* sm, int pb) {
  const int tid = threadIdx.x, w = __builtin_amdgcn_readfirstlane(tid >> 6), l = tid & 63;
  const int lt = pb >> 3, dtile = pb & 7;
  ST(p.ws, 224 + pb, 0);
  if (w < 4) {
    __builtin_amdgcn_s_setprio(PP1);
    const int ch = w >> 1, n = tid & 127, d = 2 * pb + ch, o = d * N_ + n;
    const float are = bf2f(p.are[o]), aim = bf2f(p.aim[o]);
    const float bre = bf2f(p.bre[o]), bim = bf2f(p.bim[o]), cre = bf2f(p.cre[o]), cim = bf2f(p.cim[o]);
    const float dt = bfr(expf(bf2f(p.logdt[d])));
    const float zr = dt * are, zi = dt * aim;
    float wr, wi;
    cmul(cre, cim, dt * bre, dt * bim, wr, wi);
    const float m = wmax63(fmaxf(fabsf(wr), fabsf(wi)));
    int ex = (int)((unsigned)__builtin_amdgcn_readlane((int)f2u(m), 63) >> 23) & 255;
    ex = ex < 2 ? 2 : (ex > 250 ? 250 : ex);
    const float sc = u2f((unsigned)(253 - ex) << 23), isc = u2f((unsigned)(ex + 1) << 23);
    wr *= sc; wi *= sc;
    if (l == 0) *(LDS float*)(sm + PP_S + 4 * w) = isc;
    const float zl = zr * 1.44269504089f, rv = zi * 0.15915494309f;
    auto E = [&](float x, float& er, float& ei) {
      float mag = __builtin_amdgcn_exp2f(zl * x), ph = __builtin_amdgcn_fractf(rv * x);
      er = mag * __builtin_amdgcn_cosf(ph); ei = mag * __builtin_amdgcn_sinf(ph); };
    LDS char* Pb = sm + PP_P + ch * (41 * 528) + n * 4;
    LDS char* Qb = sm + PP_Q + ch * (16 * 528) + n * 4;
    auto stP = [&](int row, float r, float i) { *(LDS unsigned*)(Pb + row * 528) = pk_f16(r, i); };
    auto stQ = [&](int row, float r, float i) { *(LDS unsigned*)(Qb + row * 528) = pk_f16(r, -i); };
    {
      float qr[16], qi[16];
      qr[0] = 1.f; qi[0] = 0.f; E(8.f, qr[1], qi[1]);
#pragma unroll
      for (int j = 2; j < 16; ++j) cmul(qr[j / 2], qi[j / 2], qr[j - j / 2], qi[j - j / 2], qr[j], qi[j]);
#pragma unroll
      for (int j = 0; j < 16; ++j) stQ(j, qr[j], qi[j]);
    }
    float e128r, e128i, e256r, e256i, e512r, e512i, e1kr, e1ki, e2kr, e2ki;
    E(128.f, e128r, e128i); E(256.f, e256r, e256i); E(512.f, e512r, e512i); E(1024.f, e1kr, e1ki); E(2048.f, e2kr, e2ki);
    float ar[16], ai[16];
    {
      float er[8], ei[8];
      er[0] = 1.f; ei[0] = 0.f; E(1.f, er[1], ei[1]);
#pragma unroll
      for (int j = 2; j < 8; ++j) cmul(er[j / 2], ei[j / 2], er[j - j / 2], ei[j - j / 2], er[j], ei[j]);
      ar[0] = wr; ai[0] = wi;
#pragma unroll
      for (int j = 1; j < 8; ++j) cmul(wr, wi, er[j], ei[j], ar[j], ai[j]);
#pragma unroll
      for (int j = 0; j < 8; ++j) cmul(ar[j], ai[j], e128r, e128i, ar[8 + j], ai[8 + j]);
#pragma unroll
      for (int j = 0; j < 16; ++j) stP(j, ar[j], ai[j]);
    }
    float br[8], bi[8];
#pragma unroll
    for (int j = 0; j < 8; ++j) { cmul(ar[2 * (j & 3) + 8 * (j >> 2)], ai[2 * (j & 3) + 8 * (j >> 2)], e256r, e256i, br[j], bi[j]); stP(16 + j, br[j], bi[j]); }
    float cr[8], ci[8];
#pragma unroll
    for (int j = 0; j < 8; ++j) {
      const int e = j & 1, ip = j >> 1;
      float sr = ip < 2 ? ar[4 * e + 8 * ip] : br[2 * e + 4 * (ip - 2)], si = ip < 2 ? ai[4 * e + 8 * ip] : bi[2 * e + 4 * (ip - 2)];
      cmul(sr, si, e512r, e512i, cr[j], ci[j]); stP(24 + j, cr[j], ci[j]); }
#pragma unroll
    for (int j = 0; j < 8; ++j) {
      float sr = j < 2 ? ar[8 * j] : (j < 4 ? br[4 * (j - 2)] : cr[2 * (j - 4)]);
      float si = j < 2 ? ai[8 * j] : (j < 4 ? bi[4 * (j - 2)] : ci[2 * (j - 4)]);
      float r, i; cmul(sr, si, e1kr, e1ki, r, i); stP(32 + j, r, i); }
    { float r, i; cmul(wr, wi, e2kr, e2ki, r, i); stP(40, r, i); }
  } else {
    const int t = tid - 256, wv = w - 4;
    const unsigned scr = (unsigned)(size_t)sm + PP_S + 64;
    if (wv == 3 && pb < CTB) {
      unsigned long long pc; unsigned o1, o2;
      asm volatile("s_getpc_b64 %0\n.Lct%=:\n\ts_mov_b32 %1, .Lct%=-_Z2k12P1\n\ts_mov_b32 %2, _Z2k22P2-_Z2k12P1" : "=s"(pc), "=s"(o1), "=s"(o2));
#pragma unroll
      for (int i = 0; i < 4; ++i) {
        const unsigned o = (unsigned)(i * 64 + l) * 128u;
        if (o < o2 + CTK2) dma4v((const char*)(pc - o1) + o, scr);
      }
    }
    const u16* tw = ((l & 31) < 14 ? p.wg : p.wu) + (size_t)(lt * F_ + 896 * (pb & 1) + ((l & 31) < 14 ? (l & 31) : (l & 31) - 14) * 64);
    if (wv < 2) { if ((l & 31) < 28) dma4v(tw + (size_t)(32 * (TS0 + 2 * wv + (l >> 5))) * F_, scr); }
    else if (wv == 2) dma4(2u * ((512 * ((pb & 7) >> 1) + 16 * lt + (l >> 3)) * D_) + (l & 7) * 128, (const char*)p.u, scr);
    else dma4(2u * ((512 * ((pb & 7) >> 1) + 16 * lt + 8 + (l >> 3)) * D_) + (l & 7) * 128, (const char*)p.u, scr);
    const u32x4 ut0 = *(const u32x4*)(p.u + (size_t)(64 * lt + (t >> 3)) * D_ + 64 * dtile + 8 * (t & 7));
    const u32x4 ut1 = *(const u32x4*)(p.u + (size_t)(64 * lt + 32 + (t >> 3)) * D_ + 64 * dtile + 8 * (t & 7));
#pragma unroll
    for (int k = 0; k < 2; ++k) {
      const int i = wv + 4 * k, f = 7 * pb + i;
      if (i < 7) glds16(p.wd + (size_t)(16 * (f >> 4) + (l >> 2)) * D_ + 32 * (f & 15) + 8 * (l & 3), sm + PP_W + i * 1024);
    }
    const u32x4 gwv = *(const u32x4*)(p.gw + 8 * l);
    const u32x4 gx0 = *(const u32x4*)(p.u + (size_t)(8 * pb + 2 * wv) * D_ + 8 * l);
    const u32x4 gx1 = *(const u32x4*)(p.u + (size_t)(8 * pb + 2 * wv + 1) * D_ + 8 * l);
    {
      const u32x4 ones = {~0u, ~0u, ~0u, ~0u};
      st16wt(p.ws, (unsigned)WS_AT + (unsigned)(pb * 8192 + t * 16), ones);
      st16wt(p.ws, (unsigned)WS_AT + (unsigned)(pb * 8192 + 4096 + t * 16), ones);
    }
    float s0 = 0.f, s1 = 0.f;
#pragma unroll
    for (int e = 0; e < 4; ++e) {
      const float gl = bflo(gwv[e]), gh = bfhi(gwv[e]);
      unsigned p0 = pk_bf16(bflo(gx0[e]) * gl, bfhi(gx0[e]) * gh), p1 = pk_bf16(bflo(gx1[e]) * gl, bfhi(gx1[e]) * gh);
      s0 += bflo(p0); s0 += bfhi(p0); s1 += bflo(p1); s1 += bfhi(p1);
    }
    s0 = wsum63(s0); s1 = wsum63(s1);
    if (l == 63) {
      f32x2 gg = {bfr(sigmoidf_(bfr(s0))), bfr(sigmoidf_(bfr(s1)))};
      *(f32x2*)(p.ws + WS_G + (8 * pb + 2 * wv) * 4) = gg;
    }
#pragma unroll
    for (int e = 0; e < 4; ++e) {
      *(LDS unsigned*)(sm + PP_T + (t >> 3) * 132 + (t & 7) * 16 + 4 * e) = ut0[e];
      *(LDS unsigned*)(sm + PP_T + (32 + (t >> 3)) * 132 + (t & 7) * 16 + 4 * e) = ut1[e];
    }
    if (wv == 3) {
      for (int s = TS0 + 4; s < TS0 + 4 + TSPLIT; ++s) {
        if (l < 28) dma4v(tw + (size_t)(32 * s) * F_, scr);
        __builtin_amdgcn_s_sleep(TSLP);
      }
      asm volatile("s_waitcnt vmcnt(%0)" :: "n"(TSPLIT) : "memory");
    } else
    WAIT_V(0);
  }
  ST(p.ws, 224 + pb, 1);
  syncthreads();
  __builtin_amdgcn_s_setprio(PP2);
  if (w < 6) {
    const int c2 = w >= 3, T = w - 3 * c2;
    LDS char* pa = sm + PP_P + c2 * (41 * 528) + (16 * T + (l & 15)) * 528 + (l >> 4) * 16;
    LDS char* pq = sm + PP_Q + c2 * (16 * 528) + (l & 15) * 528 + (l >> 4) * 16;
    f32x4 acc0 = {0.f, 0.f, 0.f, 0.f}, acc1 = {0.f, 0.f, 0.f, 0.f};
#pragma unroll
    for (int s = 0; s < 4; ++s) {
      acc0 = __builtin_amdgcn_mfma_f32_16x16x32_f16(*(LDS f16x8*)(pa + s * 64), *(LDS f16x8*)(pq + s * 64), acc0, 0, 0, 0);
      acc1 = __builtin_amdgcn_mfma_f32_16x16x32_f16(*(LDS f16x8*)(pa + 256 + s * 64), *(LDS f16x8*)(pq + 256 + s * 64), acc1, 0, 0, 0);
    }
    const float is0 = *(LDS float*)(sm + PP_S + 8 * c2), is1 = *(LDS float*)(sm + PP_S + 8 * c2 + 4);
    LDS float* kc = (LDS float*)(sm + PP_KC) + c2 * 656;
    const int j = l & 15;
#pragma unroll
    for (int r = 0; r < 4; ++r) {
      const int row = 4 * (l >> 4) + r;
      int ci_;
      if (T == 0) ci_ = (row & 7) + 128 * (row >> 3) + 8 * j;
      else if (T == 1) ci_ = row < 8 ? 256 + (row & 3) + 64 * (row >> 2) + 4 * j : 384 + (row & 1) + 32 * ((row - 8) >> 1) + 2 * j;
      else ci_ = row < 8 ? 512 + 16 * row + j : ((row == 8 && j == 0) ? 640 : 641);
      kc[ci_] = acc0[r] * is0 + acc1[r] * is1;
    }
  }
  if (w >= 4) {
    const int t = tid - 256;
    if (w == TW2) {
      const unsigned scr = (unsigned)(size_t)sm + PP_S + 64;
      const u16* tw = ((l & 31) < 14 ? p.wg : p.wu) + (size_t)(lt * F_ + 896 * (pb & 1) + ((l & 31) < 14 ? (l & 31) : (l & 31) - 14) * 64);
      for (int s = TS0 + 4 + TSPLIT + (l >> 5); s < 16; s += 2) {
        if ((l & 31) < 28) dma4v(tw + (size_t)(32 * s) * F_, scr);
        __builtin_amdgcn_s_sleep(TSLP2);
      }
    }
    if (w >= 6) {
      const int h = l >> 5, blk = (l >> 4) & 1, q = (l & 15) >> 2, pp = l & 3;
#pragma unroll
      for (int k = 0; k < 4; ++k) {
        const int i = 4 * (w - 6) + k, f = 7 * pb + i;
        if (i < 7) {
          LDS char* b0 = sm + PP_W + i * 1024 + (4 * h + q) * 64 + (16 * blk + 4 * pp) * 2;
          s16x4 a = trread(b0), b = trread(b0 + 8 * 64);
          s16x8 r = __builtin_shufflevector(a, b, 0, 1, 2, 3, 4, 5, 6, 7);
          st16wt(p.ws, (unsigned)WS_WD + (unsigned)(((f & 15) * KS2 + (f >> 4)) * 1024 + l * 16), __builtin_bit_cast(u32x4, r));
        }
      }
    }
#pragma unroll
    for (int k = 0; k < 2; ++k) {
      const int dd = (t >> 3) + 32 * k, lc = (t & 7) * 8;
      u32x4 o4;
#pragma unroll
      for (int e = 0; e < 4; ++e) {
        u16 a = *(LDS u16*)(sm + PP_T + (lc + 2 * e) * 132 + 2 * dd), b = *(LDS u16*)(sm + PP_T + (lc + 2 * e + 1) * 132 + 2 * dd);
        o4[e] = (unsigned)a | ((unsigned)b << 16);
      }
      st16wt(p.ws, (unsigned)WS_UT + (unsigned)(((64 * dtile + dd) * L_ + 64 * lt + lc) * 2), o4);
    }
  }
  syncthreads();
  ST(p.ws, 224 + pb, 2);
  {
    const int c2 = w >> 2, c = tid & 255, k = 255 - c, cls = w & 3;
    LDS float* kc = (LDS float*)(sm + PP_KC) + c2 * 656;
    u32x4 ov;
    if (cls < 2) {
      const float lo = kc[384 + k], hi = kc[385 + k];
      const unsigned hh = pk_bf16(hi, hi), ll = pk_bf16(lo, lo);
      ov[0] = hh; ov[1] = (k & 1) ? hh : ((hh & 0xffffu) | (ll & 0xffff0000u)); ov[2] = ll; ov[3] = ll;
    } else if (cls == 2) {
      const float x0 = kc[256 + 2 * k], x1 = kc[257 + 2 * k], x2 = kc[258 + 2 * k];
      ov[0] = pk_bf16(x2, x2); ov[1] = pk_bf16(x1, x1); ov[2] = pk_bf16(x1, x0); ov[3] = pk_bf16(x0, x0);
    } else {
      const bool c_ = l < 32;
      LDS float* b = kc + (c_ ? 128 + 4 * k : 8 * k);
      const float v0 = b[c_ ? 4 : 7], v1 = b[c_ ? 3 : 6], v2 = b[c_ ? 2 : 5], v3 = b[c_ ? 2 : 4];
      const float v4 = b[c_ ? 2 : 3], v5 = b[c_ ? 1 : 2], v6 = b[c_ ? 0 : 1], v7 = b[0];
      ov[0] = pk_bf16(v0, v1); ov[1] = pk_bf16(v2, v3); ov[2] = pk_bf16(v4, v5); ov[3] = pk_bf16(v6, v7);
    }
    st16wt(p.ws, (unsigned)WS_KR + (unsigned)(((2 * pb + c2) * KROW + 8 * c) * 2), ov);
    if (tid < 8) { u32x4 z = {0u, 0u, 0u, 0u}; st16wt(p.ws, (unsigned)WS_KR + (unsigned)(((2 * pb + (tid >> 2)) * KROW + 2048 + 8 * (tid & 3)) * 2), z); }
  }
  WAIT_V(0);
  ST(p.ws, 224 + pb, 3);
}

__global__ void __launch_bounds__(512, 4) k1(P1 p) {
  extern __shared__ __attribute__((aligned(16))) char smem[];
  LDS char* sm = (LDS char*)smem;
  if (blockIdx.x < 224) gemm1(p, sm, blockIdx.x); else prep(p, sm, blockIdx.x - 224);
}

struct P2 { const u16* dskip; u16* out; char* ws; };
constexpr int CV_K = 0;
constexpr int CV_U = 2 * 4 * 4160;
constexpr int CV_R = CV_U + 2 * 4608;
constexpr int K2_LDS = 65536 + 8192;

__device__ __forceinline__ void conv(const P2& p, LDS char* sm, int cb) {
  const int tid = threadIdx.x, w = __builtin_amdgcn_readfirstlane(tid >> 6), l = tid & 63;
  ST(p.ws, 512 + cb, 0);
  __builtin_amdgcn_s_setprio(3);
  const u16* ut = (const u16*)(p.ws + WS_UT) + (size_t)(2 * cb) * L_;
  const u16* kr = (const u16*)(p.ws + WS_KR) + (size_t)(2 * cb) * KROW;
  u32x4 uv[2];
#pragma unroll
  for (int c = 0; c < 2; ++c) uv[c] = *(const u32x4*)(ut + c * L_ + 8 * tid);
  u32x4 k0[3]; u32x2 k1v[3];
#pragma unroll
  for (int i = 0; i < 3; ++i) {
    int job = tid + 256 * i; job = job < 520 ? job : 519;
    const int c2 = job >= 260, c = job - 260 * c2;
    k0[i] = *(const u32x4*)(kr + c2 * KROW + 8 * c);
    k1v[i] = u32x2{0u, 0u};
    if (l == 63) k1v[i] = *(const u32x2*)(kr + c2 * KROW + 8 * c + 8);
  }
  const float dsk = bf2f(p.dskip[2 * cb + (w >> 1)]);
#pragma unroll
  for (int c = 0; c < 2; ++c) {
    *(LDS u32x4*)(sm + CV_U + c * 4608 + 512 + 16 * tid) = uv[c];
    if (tid < 32) { u32x4 z = {0u, 0u, 0u, 0u}; *(LDS u32x4*)(sm + CV_U + c * 4608 + 16 * tid) = z; }
  }
#pragma unroll
  for (int i = 0; i < 3; ++i) {
    const int job = tid + 256 * i;
    if (job < 520) {
      const int c2 = job >= 260, c = job - 260 * c2;
      const unsigned n0 = (unsigned)__builtin_amdgcn_update_dpp((int)k1v[i][0], (int)k0[i][0], 0x130, 0xf, 0xf, false);
      const unsigned n1 = (unsigned)__builtin_amdgcn_update_dpp((int)k1v[i][1], (int)k0[i][1], 0x130, 0xf, 0xf, false);
      unsigned dd[6] = {k0[i][0], k0[i][1], k0[i][2], k0[i][3], c == 259 ? 0u : n0, c == 259 ? 0u : n1};
      LDS char* b = sm + CV_K + c2 * (4 * 4160) + 16 * c;
      u32x4 c0 = {dd[0], dd[1], dd[2], dd[3]}, c2v = {dd[1], dd[2], dd[3], dd[4]}, c1, c3;
#pragma unroll
      for (int e = 0; e < 4; ++e) { c1[e] = __builtin_amdgcn_alignbit(dd[e + 1], dd[e], 16); c3[e] = __builtin_amdgcn_alignbit(dd[e + 2], dd[e + 1], 16); }
      *(LDS u32x4*)(b) = c0; *(LDS u32x4*)(b + 4160) = c1; *(LDS u32x4*)(b + 2 * 4160) = c2v; *(LDS u32x4*)(b + 3 * 4160) = c3;
    }
  }
  syncthreads();
  ST(p.ws, 512 + cb, 1);
  const int ch = w >> 1, rh = w & 1, j = l & 15, g = l >> 4;
  const int e = 3 - (j & 3);
  LDS char* ka = sm + CV_K + ch * (4 * 4160) + e * 4160 + 2 * (2031 - j + 8 * g - e);
  LDS char* ub = sm + CV_U + ch * 4608 + 2 * (240 + 16 * j + 8 * g);
  f32x4 acc[8];
#pragma unroll
  for (int c = 0; c < 8; ++c) acc[c] = f32x4{0.f, 0.f, 0.f, 0.f};
#pragma unroll
  for (int rr = 0; rr < 4; ++rr) {
    const int r = 4 * rh + rr;
    bf16x8 bf[8];
#pragma unroll
    for (int t = 0; t < 8; ++t) bf[t] = *(LDS bf16x8*)(ub + 512 * t - 64 * CVR);
#pragma unroll
    for (int q = 0; q < 8; ++q) {
      LDS char* a = ka - 64 * (8 * CVQ + CVR);
      s16x4 lo = *(LDS s16x4*)a, hi = *(LDS s16x4*)(a + 8);
      bf16x8 af = cat8(lo, hi);
#pragma unroll
      for (int c = q; c < 8; ++c) acc[c] = __builtin_amdgcn_mfma_f32_16x16x32_bf16(af, bf[c - q], acc[c], 0, 0, 0);
    }
  }
  ST(p.ws, 512 + cb, 2);
  LDS char* rb = sm + CV_R;
#pragma unroll
  for (int c = 0; c < 4; ++c) *(LDS f32x4*)(rb + (w * 4 + c) * 1024 + l * 16) = acc[4 * (rh ^ 1) + c];
  syncthreads();
  u16* at = (u16*)(p.ws + WS_AT) + (size_t)(2 * cb + ch) * L_;
#pragma unroll
  for (int c = 0; c < 4; ++c) {
    f32x4 o = *(LDS f32x4*)(rb + ((w ^ 1) * 4 + c) * 1024 + l * 16);
    f32x4 y = acc[4 * rh + c] + o;
    const int lpos = 256 * (4 * rh + c) + 16 * j + 4 * g;
    u32x2 uu = *(LDS u32x2*)(sm + CV_U + ch * 4608 + 512 + 2 * lpos);
    unsigned y0 = pk_bf16(y[0], y[1]), y1 = pk_bf16(y[2], y[3]);
    unsigned s0 = pk_bf16(bflo(uu[0]) * dsk, bfhi(uu[0]) * dsk), s1 = pk_bf16(bflo(uu[1]) * dsk, bfhi(uu[1]) * dsk);
    unsigned a0 = pk_bf16(bflo(y0) + bflo(s0), bfhi(y0) + bfhi(s0)), a1 = pk_bf16(bflo(y1) + bflo(s1), bfhi(y1) + bfhi(s1));
    unsigned long long v = (unsigned long long)a0 | ((unsigned long long)a1 << 32);
    if (v == ~0ull) v = 0x7FC07FC07FC07FC0ull;
    __hip_atomic_store((GLB unsigned long long*)(at + lpos), v, __ATOMIC_RELAXED, __HIP_MEMORY_SCOPE_AGENT);
  }
  ST(p.ws, 512 + cb, 3);
}

#define PFX 9
constexpr int PF = PFX;
#define APRE 4
struct F2 { bf16x8 a0, a1, b0, b1; };
#define GLD16(dst, voff, sbase, off) asm volatile("global_load_dwordx4 %0, %1, %2 offset:%3" : "=v"(dst) : "v"(voff), "s"(sbase), "i"(off))
template <int S> __device__ __forceinline__ void g2_ld(F2& f, unsigned vo, const char* A0, const char* B0) {
  const char *a = A0 + (S >> 2) * 4096, *b = B0 + (S >> 2) * 4096;
  GLD16(f.a0, vo, a, (S & 3) * 1024); GLD16(f.b0, vo, b, (S & 3) * 1024);
  GLD16(f.a1, vo, a + KS2 * 1024, (S & 3) * 1024); GLD16(f.b1, vo, b + KS2 * 1024, (S & 3) * 1024); }
template <int S> __device__ __forceinline__ void g2_pro(F2 (&f)[PF], unsigned vo, const char* A0, const char* B0) {
  g2_ld<S>(f[S], vo, A0, B0);
  if constexpr (S + 1 < PF) g2_pro<S + 1>(f, vo, A0, B0); }
template <int S> __device__ __forceinline__ void g2_step(F2 (&f)[PF], unsigned vo, const char* A0, const char* B0, f32x16 (&acc)[2][2],
                                                         unsigned va, const char* AT, unsigned long long (&av8)[4]) {
  F2& c = f[S % PF];
  constexpr int pend = (S + PF <= 28 ? PF : 28 - S) - 1;
  asm volatile("s_waitcnt vmcnt(%4)" : "+v"(c.a0), "+v"(c.a1), "+v"(c.b0), "+v"(c.b1) : "i"(4 * pend + (S + PF > 27 + APRE ? 4 : 0)) : "memory");
  acc[0][0] = MFMA32(c.a0, c.b0, acc[0][0]); acc[0][1] = MFMA32(c.a0, c.b1, acc[0][1]);
  acc[1][0] = MFMA32(c.a1, c.b0, acc[1][0]); acc[1][1] = MFMA32(c.a1, c.b1, acc[1][1]);
  SCHED();
  if constexpr (S + PF < 28) g2_ld<S + PF>(c, vo, A0, B0);
  if constexpr (S + PF == 27 + APRE) {
#pragma unroll
    for (int i = 0; i < 4; ++i)
      asm volatile("global_load_dwordx2 %0, %1, %2 sc1" : "=v"(av8[i]) : "v"(va), "s"(AT + (size_t)i * 16 * L_ * 2) : "memory");
  }
  SCHED();
  if constexpr (S + 1 < 28) g2_step<S + 1>(f, vo, A0, B0, acc, va, AT, av8);
}
__device__ __forceinline__ void gemm2(const P2& p, LDS char* sm, int b) {
  const int tid = threadIdx.x, w = __builtin_amdgcn_readfirstlane(tid >> 6), l = tid & 63;
  const int xcd = b & 7, idx = b >> 3;
  const int mt = 4 * xcd + (idx & 3), nt = idx >> 2;
#define K2NT nt
#define K2MT mt
  const char* A0 = p.ws + WS_WD + ((size_t)(2 * (K2NT)) * KS2 + 28 * w) * 1024;
  const char* B0 = p.ws + WS_H + ((size_t)(2 * (K2MT)) * KS2 + 28 * w) * 1024;
  f32x16 acc[2][2];
#pragma unroll
  for (int i = 0; i < 2; ++i)
#pragma unroll
    for (int j = 0; j < 2; ++j)
#pragma unroll
      for (int r = 0; r < 16; ++r) acc[i][j][r] = 0.f;
  ST(p.ws, 768 + b, 0);
  __builtin_amdgcn_s_setprio(G2P);
  const int tl = 16 * w + (l & 15), tok = 64 * mt + tl;
  float g;
  asm volatile("global_load_dword %0, %1, %2" : "=v"(g) : "v"(tok * 4), "s"(p.ws + WS_G));
  F2 f[PF];
  g2_pro<0>(f, l * 16, A0, B0);
  SCHED();
  unsigned long long av8[4];
  g2_step<0>(f, l * 16, A0, B0, acc, (unsigned)(((tid >> 4) * L_ + 4 * (tid & 15)) * 2),
             p.ws + WS_AT + ((size_t)(64 * nt) * L_ + 64 * mt) * 2, av8);
  asm volatile("s_waitcnt vmcnt(0)" : "+v"(g), "+v"(av8[0]), "+v"(av8[1]), "+v"(av8[2]), "+v"(av8[3]) :: "memory");
  ST(p.ws, 768 + b, 1);
  GLB unsigned long long* at = (GLB unsigned long long*)((const u16*)(p.ws + WS_AT) + (size_t)(64 * nt + (tid >> 4)) * L_ + 64 * mt + 4 * (tid & 15));
#pragma unroll
  for (int i = 0; i < 2; ++i)
#pragma unroll
    for (int j = 0; j < 2; ++j)
#pragma unroll
      for (int q = 0; q < 4; ++q) {
        f32x4 v = {acc[i][j][4 * q], acc[i][j][4 * q + 1], acc[i][j][4 * q + 2], acc[i][j][4 * q + 3]};
        *(LDS f32x4*)(sm + w * 16384 + ((2 * i + j) * 4 + q) * 1024 + l * 16) = v;
      }
  for (unsigned spins = 0; spins < (1u << 22); ++spins) {
    const bool bad = av8[0] == ~0ull || av8[1] == ~0ull || av8[2] == ~0ull || av8[3] == ~0ull;
    if (__builtin_amdgcn_ballot_w64(bad) == 0ull) break;
    __builtin_amdgcn_s_sleep(1);
#pragma unroll
    for (int i = 0; i < 4; ++i) av8[i] = __hip_atomic_load(at + (size_t)i * 16 * L_ / 4, __ATOMIC_RELAXED, __HIP_MEMORY_SCOPE_AGENT);
  }
#pragma unroll
  for (int i = 0; i < 4; ++i) *(LDS unsigned long long*)(sm + 65536 + ((tid >> 4) + 16 * i) * 128 + (tid & 15) * 8) = av8[i];
  syncthreads();
  ST(p.ws, 768 + b, 2);
  const float og = bfr(1.f - g);
  const int q = l >> 4, qq = (l & 15) >> 2, pp = l & 3;
#pragma unroll
  for (int k = 0; k < 2; ++k) {
    unsigned ov[4];
#pragma unroll
    for (int h = 0; h < 2; ++h) {
      LDS char* rp = sm + ((2 * k + (w >> 1)) * 4 + q) * 1024 + (16 * (w & 1) + (l & 15) + 32 * h) * 16;
      f32x4 s = *(LDS f32x4*)rp;
#pragma unroll
      for (int ww = 1; ww < 4; ++ww) s += *(LDS f32x4*)(rp + ww * 16384);
      s16x4 av = trread(sm + 65536 + (32 * k + 8 * q + 4 * h + qq) * 128 + (16 * w + 4 * pp) * 2);
      float o[4];
#pragma unroll
      for (int e = 0; e < 4; ++e) o[e] = bfr(g * bf2f((u16)av[e])) + bfr(og * bfr(s[e]));
      ov[2 * h] = pk_bf16(o[0], o[1]); ov[2 * h + 1] = pk_bf16(o[2], o[3]);
    }
    u32x4 o4 = {ov[0], ov[1], ov[2], ov[3]};
    __builtin_amdgcn_raw_buffer_store_b128(o4, __builtin_amdgcn_make_buffer_rsrc(p.out, 0, L_ * D_ * 2, 0x00020000),
                                           (unsigned)((tok * D_ + 64 * nt + 32 * k + 8 * q) * 2), 0, OWT);
  }
  ST(p.ws, 768 + b, 3);
}

__global__ void __launch_bounds__(256, 2) k2(P2 p) {
  extern __shared__ __attribute__((aligned(16))) char smem[];
  LDS char* sm = (LDS char*)smem;
  if (blockIdx.x < 256) conv(p, sm, blockIdx.x); else gemm2(p, sm, blockIdx.x - 256);
}

#if defined(__HIP_DEVICE_COMPILE__)
#pragma clang attribute pop
#endif
extern "C" void kernel_launch(void* const* d_in, const int* in_sizes, int n_in, void* d_out, int out_size,
                              void* d_ws, size_t ws_size, hipStream_t stream) {
  P1 a;
  a.u = (const u16*)d_in[0]; a.are = (const u16*)d_in[1]; a.aim = (const u16*)d_in[2]; a.bre = (const u16*)d_in[3];
  a.bim = (const u16*)d_in[4]; a.cre = (const u16*)d_in[5]; a.cim = (const u16*)d_in[6]; a.logdt = (const u16*)d_in[7];
  a.wg = (const u16*)d_in[10]; a.wu = (const u16*)d_in[11]; a.wd = (const u16*)d_in[12];
  a.gw = (const u16*)d_in[13]; a.ws = (char*)d_ws;
  P2 b; b.dskip = (const u16*)d_in[8]; b.out = (u16*)d_out; b.ws = (char*)d_ws;
  (void)hipFuncSetAttribute((const void*)k1, hipFuncAttributeMaxDynamicSharedMemorySize, 81920);
  (void)hipFuncSetAttribute((const void*)k2, hipFuncAttributeMaxDynamicSharedMemorySize, K2_LDS);
  hipLaunchKernelGGL(k1, dim3(480), dim3(512), 81920, stream, a);
  hipLaunchKernelGGL(k2, dim3(512), dim3(256), K2_LDS, stream, b);
}
```

```cpp
#include <hip/hip_runtime.h>
#include <hip/hip_bf16.h>
#include <stdint.h>

#if defined(__HIP_DEVICE_COMPILE__)
#pragma clang attribute push(__attribute__((target("no-packed-fp32-ops"))), apply_to = function)
#endif
typedef unsigned short u16;
typedef __attribute__((ext_vector_type(4))) short s16x4;
typedef __attribute__((ext_vector_type(8))) short s16x8;
typedef __attribute__((ext_vector_type(8))) __bf16 bf16x8;
typedef __attribute__((ext_vector_type(2))) __bf16 bf16x2;
typedef __attribute__((ext_vector_type(8))) _Float16 f16x8;
typedef __attribute__((ext_vector_type(2))) _Float16 f16x2;
typedef __attribute__((ext_vector_type(16))) float f32x16;
typedef __attribute__((ext_vector_type(4))) float f32x4;
typedef __attribute__((ext_vector_type(2))) float f32x2;
typedef __attribute__((ext_vector_type(4))) unsigned u32x4;
typedef __attribute__((ext_vector_type(2))) unsigned u32x2;
#define LDS __attribute__((address_space(3)))
#define GLB __attribute__((address_space(1)))

constexpr int L_ = 2048, D_ = 512, N_ = 128, F_ = 1792;
constexpr int KS2 = F_ / 16;
constexpr int KROW = 2080;
constexpr size_t WS_H    = 0;
constexpr size_t WS_WD   = WS_H + (size_t)64 * KS2 * 1024;
constexpr size_t WS_UT   = WS_WD + (size_t)16 * KS2 * 1024;
constexpr size_t WS_KR   = WS_UT + (size_t)D_ * L_ * 2;
constexpr size_t WS_AT   = WS_KR + (size_t)D_ * KROW * 2 + 4096;
constexpr size_t WS_G    = WS_AT + (size_t)D_ * L_ * 2;
constexpr unsigned WS_BYTES = (unsigned)(WS_G + (size_t)L_ * 4);

__device__ __forceinline__ float u2f(unsigned v) { return __builtin_bit_cast(float, v); }
__device__ __forceinline__ unsigned f2u(float v) { return __builtin_bit_cast(unsigned, v); }
__device__ __forceinline__ void syncthreads() {
  __builtin_amdgcn_fence(__ATOMIC_RELEASE, "workgroup"); __builtin_amdgcn_s_barrier(); __builtin_amdgcn_fence(__ATOMIC_ACQUIRE, "workgroup"); }
__device__ __forceinline__ float shx(float v, int lane, int m) {
  return u2f((unsigned)__builtin_amdgcn_ds_bpermute((lane ^ m) << 2, (int)f2u(v))); }
__device__ __forceinline__ float bf2f(u16 v) { return u2f((unsigned)v << 16); }
__device__ __forceinline__ unsigned pk_bf16(float a, float b) {
  f32x2 t = {a, b}; bf16x2 r = __builtin_convertvector(t, bf16x2); return __builtin_bit_cast(unsigned, r); }
__device__ __forceinline__ float bflo(unsigned p) { return u2f(p << 16); }
__device__ __forceinline__ float bfhi(unsigned p) { return u2f(p & 0xffff0000u); }
__device__ __forceinline__ float bfr(float a) { return bflo(pk_bf16(a, 0.f)); }
__device__ __forceinline__ unsigned pk_f16(float a, float b) {
  f32x2 t = {a, b}; f16x2 r = __builtin_convertvector(t, f16x2); return __builtin_bit_cast(unsigned, r); }
__device__ __forceinline__ float sigmoidf_(float x) { return __builtin_amdgcn_rcpf(1.f + __builtin_amdgcn_exp2f(-1.44269504089f * x)); }
#define WAIT_V(n) asm volatile("s_waitcnt vmcnt(" #n ")" ::: "memory")
#define WAIT_L(n) asm volatile("s_waitcnt lgkmcnt(" #n ")" ::: "memory")
#define BAR() __builtin_amdgcn_s_barrier()
#define SCHED() __builtin_amdgcn_sched_barrier(0)
#define ST(ws, row, i)
__device__ __forceinline__ void glds16(const void* g, LDS char* l) {
  __builtin_amdgcn_global_load_lds((const GLB void*)g, (LDS void*)l, 16, 0, 0); }

__device__ __forceinline__ void st16wt(char* ws, unsigned off, u32x4 v) {
  __builtin_amdgcn_raw_buffer_store_b128(v, __builtin_amdgcn_make_buffer_rsrc(ws, 0, WS_BYTES, 0x00020000), off, 0, 16); }
__device__ __forceinline__ s16x4 trread(LDS char* p) { return __builtin_amdgcn_ds_read_tr16_b64_v4i16((LDS s16x4*)p); }
__device__ __forceinline__ bf16x8 cat8(s16x4 a, s16x4 b) {
  s16x8 r = __builtin_shufflevector(a, b, 0, 1, 2, 3, 4, 5, 6, 7); return __builtin_bit_cast(bf16x8, r); }

struct P1 {
  const u16 *u, *are, *aim, *bre, *bim, *cre, *cim, *logdt, *wg, *wu, *wd, *gw;
  char* ws;
};

#define DSR_TR(dst, addr, off) asm volatile("ds_read_b64_tr_b16 %0, %1 offset:%2" : "=v"(dst) : "v"(addr), "i"(off))
#define DSR_128(dst, addr, off) asm volatile("ds_read_b128 %0, %1 offset:%2" : "=v"(dst) : "v"(addr), "i"(off))
__device__ __forceinline__ void dma16(unsigned voff, const char* sbase, unsigned ldsb) {
  asm volatile("s_mov_b32 m0, %2\n\ts_nop 0\n\tglobal_load_lds_dwordx4 %0, %1" :: "v"(voff), "s"(sbase), "s"(ldsb) : "memory"); }
__device__ __forceinline__ void dma4(unsigned voff, const char* sbase, unsigned ldsb) {
  asm volatile("s_mov_b32 m0, %2\n\ts_nop 0\n\tglobal_load_lds_dword %0, %1" :: "v"(voff), "s"(sbase), "s"(ldsb) : "memory"); }
struct Frag { s16x4 a0l, a0h, a1l, a1h; bf16x8 b0, b1; };
#define FRAG_WAIT(f) asm volatile("s_waitcnt lgkmcnt(0)" : "+v"(f.a0l), "+v"(f.a0h), "+v"(f.a1l), "+v"(f.a1h), "+v"(f.b0), "+v"(f.b1) :: "memory")
#define MFMA32(a, b, c) __builtin_amdgcn_mfma_f32_32x32x16_bf16(a, b, c, 0, 0, 0)
struct G1 {
  unsigned a0, a1, bx[4];
  unsigned vg, vu, vx0, vx1;
  const char *wg, *wu, *x;
  unsigned lw, lx;
};
template <int S, int KS> __device__ __forceinline__ void g1_rd0(const G1& g, Frag& f) {
  constexpr int wo = (S % 3) * 16384 + KS * 4096, xo = ((S >> 1) & 1) * 16384;
  DSR_TR(f.a0l, g.a0, wo); DSR_TR(f.a0h, g.a0, wo + 1024); DSR_128(f.b0, g.bx[2 * (S & 1) + KS], xo); }
template <int S, int KS> __device__ __forceinline__ void g1_rd1(const G1& g, Frag& f) {
  constexpr int wo = (S % 3) * 16384 + KS * 4096, xo = ((S >> 1) & 1) * 16384;
  DSR_128(f.b1, g.bx[2 * (S & 1) + KS], xo + 4096); DSR_TR(f.a1l, g.a1, wo); DSR_TR(f.a1h, g.a1, wo + 1024); }
template <int S> __device__ __forceinline__ void g1_issueW(const G1& g) {
  dma16(g.vg, g.wg + (size_t)S * 32 * F_ * 2, g.lw + (S % 3) * 16384);
  dma16(g.vu, g.wu + (size_t)S * 32 * F_ * 2, g.lw + (S % 3) * 16384 + 8192); }
template <int J> __device__ __forceinline__ void g1_issueX(const G1& g) {
  dma16(g.vx0, g.x + J * 128, g.lx + (J & 1) * 16384);
  dma16(g.vx1, g.x + J * 128, g.lx + (J & 1) * 16384 + 1024); }
template <int S> __device__ __forceinline__ void g1_stage(const G1& g, Frag& f0, Frag& f1, f32x16 (&acc)[2][2]) {
  FRAG_WAIT(f0);
  {
    const bf16x8 a0 = cat8(f0.a0l, f0.a0h), a1 = cat8(f0.a1l, f0.a1h);
    acc[0][0] = MFMA32(a0, f0.b0, acc[0][0]); SCHED();
    g1_rd0<S, 1>(g, f1); SCHED();
    acc[0][1] = MFMA32(a0, f0.b1, acc[0][1]); SCHED();
    g1_rd1<S, 1>(g, f1); SCHED();
    acc[1][0] = MFMA32(a1, f0.b0, acc[1][0]); SCHED();
    acc[1][1] = MFMA32(a1, f0.b1, acc[1][1]); SCHED();
  }
  FRAG_WAIT(f1);
  if constexpr (S < 15) {
    if constexpr (S >= 14) { WAIT_V(0); } else if constexpr ((S & 1) == 0) { WAIT_V(4); } else { WAIT_V(2); }
    BAR();
  }
  {
    const bf16x8 a0 = cat8(f1.a0l, f1.a0h), a1 = cat8(f1.a1l, f1.a1h);
    acc[0][0] = MFMA32(a0, f1.b0, acc[0][0]); SCHED();
    if constexpr (S < 15) g1_rd0<S + 1, 0>(g, f0);
    SCHED();
    acc[0][1] = MFMA32(a0, f1.b1, acc[0][1]); SCHED();
    if constexpr (S < 15) g1_rd1<S + 1, 0>(g, f0);
    SCHED();
    acc[1][0] = MFMA32(a1, f1.b0, acc[1][0]); SCHED();
    if constexpr (S + 3 < 16) g1_issueW<S + 3>(g);
    SCHED();
    acc[1][1] = MFMA32(a1, f1.b1, acc[1][1]); SCHED();
    if constexpr ((S & 1) == 1 && (S >> 1) + 2 < 8) g1_issueX<(S >> 1) + 2>(g);
    SCHED();
  }
  if constexpr (S < 15) g1_stage<S + 1>(g, f0, f1, acc);
}
__device__ __forceinline__ void gemm1(const P1& p, LDS char* sm, int b) {
  const int tid = threadIdx.x, w = __builtin_amdgcn_readfirstlane(tid >> 6), l = tid & 63;
  const int xcd = b & 7, idx = b >> 3;
  const int mt = 4 * (xcd >> 1) + (idx & 3), nt = 7 * (xcd & 1) + (idx >> 2);
  const int wm = w & 1, wn = w >> 1;
  const unsigned smb = (unsigned)(size_t)sm;
  G1 g;
  {
    const int krow = 4 * w + (l >> 4);
    g.vg = 2u * (krow * F_ + 8 * ((l & 15) ^ (2 * (l >> 4))));
    g.vu = 2u * (krow * F_ + 8 * ((l & 15) ^ (2 * ((l >> 4) | 4))));
    const int xr0 = 16 * w + (l >> 3);
    g.vx0 = 2u * (xr0 * D_ + 8 * ((l & 7) ^ ((xr0 >> 1) & 7)));
    g.vx1 = 2u * ((xr0 + 8) * D_ + 8 * ((l & 7) ^ (((xr0 + 8) >> 1) & 7)));
    g.wg = (const char*)(p.wg + nt * 128); g.wu = (const char*)(p.wu + nt * 128); g.x = (const char*)(p.u + (size_t)mt * 128 * D_);
    g.lw = smb + w * 1024; g.lx = smb + 49152 + w * 2048;
    const int h = l >> 5, blk = (l >> 4) & 1, q = (l & 15) >> 2, pp = l & 3, img = pp >> 1;
    g.a0 = smb + img * 8192 + (8 * h + q) * 256 + 32 * ((2 * wn) ^ (q | (img << 2))) + 16 * blk + 8 * (pp & 1);
    g.a1 = g.a0 ^ 32;
    const int xrow = 64 * wm + (l & 31);
    const unsigned bb = 49152 + xrow * 128 + 16 * (h ^ ((xrow >> 1) & 7));
#pragma unroll
    for (int c = 0; c < 4; ++c) g.bx[c] = smb + (bb ^ (c * 32));
  }
  f32x16 acc[2][2];
#pragma unroll
  for (int i = 0; i < 2; ++i)
#pragma unroll
    for (int j = 0; j < 2; ++j)
#pragma unroll
      for (int r = 0; r < 16; ++r) acc[i][j][r] = 0.f;
  Frag f0, f1;
  ST(p.ws, b, 0);
#define G1PRIO 2
  __builtin_amdgcn_s_setprio(G1PRIO);
  g1_issueX<0>(g); g1_issueW<0>(g); g1_issueW<1>(g); g1_issueX<1>(g); g1_issueW<2>(g);
  WAIT_V(6); BAR();
  ST(p.ws, b, 1);
  g1_rd0<0, 0>(g, f0); g1_rd1<0, 0>(g, f0);
  g1_stage<0>(g, f0, f1, acc);
  ST(p.ws, b, 2);
#pragma unroll
  for (int i = 0; i < 2; ++i)
#pragma unroll
    for (int j = 0; j < 2; ++j) {
      unsigned o[4];
#pragma unroll
      for (int hh = 0; hh < 2; ++hh) {
        float hv[4];
#pragma unroll
        for (int e = 0; e < 4; e += 2) {
          unsigned gp = pk_bf16(acc[i][j][8 * hh + e], acc[i][j][8 * hh + e + 1]);
          unsigned up = pk_bf16(acc[i][j][8 * hh + 4 + e], acc[i][j][8 * hh + 4 + e + 1]);
          float g0 = bflo(gp), g1 = bfhi(gp);
          unsigned sp = pk_bf16(g0 * sigmoidf_(g0), g1 * sigmoidf_(g1));
          hv[e] = bflo(sp) * bflo(up); hv[e + 1] = bfhi(sp) * bfhi(up);
        }
        o[2 * hh] = pk_bf16(hv[0], hv[1]); o[2 * hh + 1] = pk_bf16(hv[2], hv[3]);
      }
      const int tb = 4 * mt + 2 * wm + j, ks = 8 * nt + 2 * wn + i;
      u32x4 ov = {o[0], o[1], o[2], o[3]};
      st16wt(p.ws, (unsigned)WS_H + (unsigned)((tb * KS2 + ks) * 1024 + l * 16), ov);
    }
  ST(p.ws, b, 3);
}

constexpr int PP_P = 0;
constexpr int PP_Q = PP_P + 2 * 41 * 528;
constexpr int PP_KC = PP_Q + 2 * 16 * 528;
constexpr int PP_T = PP_KC + 2 * 656 * 4;
constexpr int PP_W = PP_T + 64 * 132;
constexpr int PP_S = PP_W + 7 * 1024;
static_assert(PP_S + 64 + 256 <= 81920, "lds");
static_assert(PP_W % 16 == 0 && PP_KC % 16 == 0 && PP_T % 16 == 0, "al");

__device__ __forceinline__ void cmul(float ar, float ai, float br, float bi, float& r, float& i) {
  r = ar * br - ai * bi; i = ar * bi + ai * br; }

#define TS0 3
#define TSLP 9
#define TSPLIT 3
#define TSLP2 4
#define PP1 3
#define PP2 3
#define CVP 3
#define G2P 0
#define CVR r
#define CVQ q
#define CVM 0
#define CT 1
#define CTB 256
#define CTK2 9216u
#define OWT 0
#define KDPP 0
#define TW2 7
template <int CTRL, int RM> __device__ __forceinline__ float dppf(float v) {
  return u2f((unsigned)__builtin_amdgcn_update_dpp(0, (int)f2u(v), CTRL, RM, 0xf, true)); }
__device__ __forceinline__ float wsum63(float v) {
  v += dppf<0x111, 0xf>(v); v += dppf<0x112, 0xf>(v); v += dppf<0x114, 0xf>(v); v += dppf<0x118, 0xf>(v);
  v += dppf<0x142, 0xa>(v); v += dppf<0x143, 0xc>(v); return v; }
__device__ __forceinline__ float wmax63(float v) {
  v = fmaxf(v, dppf<0x111, 0xf>(v)); v = fmaxf(v, dppf<0x112, 0xf>(v)); v = fmaxf(v, dppf<0x114, 0xf>(v)); v = fmaxf(v, dppf<0x118, 0xf>(v));
  v = fmaxf(v, dppf<0x142, 0xa>(v)); v = fmaxf(v, dppf<0x143, 0xc>(v)); return v; }
__device__ __forceinline__ void dma4v(const void* g, unsigned ldsb) {
  asm volatile("s_mov_b32 m0, %1\n\ts_nop 0\n\tglobal_load_lds_dword %0, off" :: "v"(g), "s"(ldsb) : "memory"); }

__device__ __forceinline__ void prep(const P1& p, LDS char* sm, int pb) {
  const int tid = threadIdx.x, w = __builtin_amdgcn_readfirstlane(tid >> 6), l = tid & 63;
  const int lt = pb >> 3, dtile = pb & 7;
  ST(p.ws, 224 + pb, 0);
  if (w < 4) {
    __builtin_amdgcn_s_setprio(PP1);
    const int ch = w >> 1, n = tid & 127, d = 2 * pb + ch, o = d * N_ + n;
    const float are = bf2f(p.are[o]), aim = bf2f(p.aim[o]);
    const float bre = bf2f(p.bre[o]), bim = bf2f(p.bim[o]), cre = bf2f(p.cre[o]), cim = bf2f(p.cim[o]);
    const float dt = bfr(expf(bf2f(p.logdt[d])));
    const float zr = dt * are, zi = dt * aim;
    float wr, wi;
    cmul(cre, cim, dt * bre, dt * bim, wr, wi);
    const float m = wmax63(fmaxf(fabsf(wr), fabsf(wi)));
    int ex = (int)((unsigned)__builtin_amdgcn_readlane((int)f2u(m), 63) >> 23) & 255;
    ex = ex < 2 ? 2 : (ex > 250 ? 250 : ex);
    const float sc = u2f((unsigned)(253 - ex) << 23), isc = u2f((unsigned)(ex + 1) << 23);
    wr *= sc; wi *= sc;
    if (l == 0) *(LDS float*)(sm + PP_S + 4 * w) = isc;
    const float zl = zr * 1.44269504089f, rv = zi * 0.15915494309f;
    auto E = [&](float x, float& er, float& ei) {
      float mag = __builtin_amdgcn_exp2f(zl * x), ph = __builtin_amdgcn_fractf(rv * x);
      er = mag * __builtin_amdgcn_cosf(ph); ei = mag * __builtin_amdgcn_sinf(ph); };
    LDS char* Pb = sm + PP_P + ch * (41 * 528) + n * 4;
    LDS char* Qb = sm + PP_Q + ch * (16 * 528) + n * 4;
    auto stP = [&](int row, float r, float i) { *(LDS unsigned*)(Pb + row * 528) = pk_f16(r, i); };
    auto stQ = [&](int row, float r, float i) { *(LDS unsigned*)(Qb + row * 528) = pk_f16(r, -i); };
    {
      float qr[16], qi[16];
      qr[0] = 1.f; qi[0] = 0.f; E(8.f, qr[1], qi[1]);
#pragma unroll
      for (int j = 2; j < 16; ++j) cmul(qr[j / 2], qi[j / 2], qr[j - j / 2], qi[j - j / 2], qr[j], qi[j]);
#pragma unroll
      for (int j = 0; j < 16; ++j) stQ(j, qr[j], qi[j]);
    }
    float e128r, e128i, e256r, e256i, e512r, e512i, e1kr, e1ki, e2kr, e2ki;
    E(128.f, e128r, e128i); E(256.f, e256r, e256i); E(512.f, e512r, e512i); E(1024.f, e1kr, e1ki); E(2048.f, e2kr, e2ki);
    float ar[16], ai[16];
    {
      float er[8], ei[8];
      er[0] = 1.f; ei[0] = 0.f; E(1.f, er[1], ei[1]);
#pragma unroll
      for (int j = 2; j < 8; ++j) cmul(er[j / 2], ei[j / 2], er[j - j / 2], ei[j - j / 2], er[j], ei[j]);
      ar[0] = wr; ai[0] = wi;
#pragma unroll
      for (int j = 1; j < 8; ++j) cmul(wr, wi, er[j], ei[j], ar[j], ai[j]);
#pragma unroll
      for (int j = 0; j < 8; ++j) cmul(ar[j], ai[j], e128r, e128i, ar[8 + j], ai[8 + j]);
#pragma unroll
      for (int j = 0; j < 16; ++j) stP(j, ar[j], ai[j]);
    }
    float br[8], bi[8];
#pragma unroll
    for (int j = 0; j < 8; ++j) { cmul(ar[2 * (j & 3) + 8 * (j >> 2)], ai[2 * (j & 3) + 8 * (j >> 2)], e256r, e256i, br[j], bi[j]); stP(16 + j, br[j], bi[j]); }
    float cr[8], ci[8];
#pragma unroll
    for (int j = 0; j < 8; ++j) {
      const int e = j & 1, ip = j >> 1;
      float sr = ip < 2 ? ar[4 * e + 8 * ip] : br[2 * e + 4 * (ip - 2)], si = ip < 2 ? ai[4 * e + 8 * ip] : bi[2 * e + 4 * (ip - 2)];
      cmul(sr, si, e512r, e512i, cr[j], ci[j]); stP(24 + j, cr[j], ci[j]); }
#pragma unroll
    for (int j = 0; j < 8; ++j) {
      float sr = j < 2 ? ar[8 * j] : (j < 4 ? br[4 * (j - 2)] : cr[2 * (j - 4)]);
      float si = j < 2 ? ai[8 * j] : (j < 4 ? bi[4 * (j - 2)] : ci[2 * (j - 4)]);
      float r, i; cmul(sr, si, e1kr, e1ki, r, i); stP(32 + j, r, i); }
    { float r, i; cmul(wr, wi, e2kr, e2ki, r, i); stP(40, r, i); }
  } else {
    const int t = tid - 256, wv = w - 4;
    const unsigned scr = (unsigned)(size_t)sm + PP_S + 64;
    if (wv == 3 && pb < CTB) {
      unsigned long long pc; unsigned o1, o2;
      asm volatile("s_getpc_b64 %0\n.Lct%=:\n\ts_mov_b32 %1, .Lct%=-_Z2k12P1\n\ts_mov_b32 %2, _Z2k22P2-_Z2k12P1" : "=s"(pc), "=s"(o1), "=s"(o2));
#pragma unroll
      for (int i = 0; i < 4; ++i) {
        const unsigned o = (unsigned)(i * 64 + l) * 128u;
        if (o < o2 + CTK2) dma4v((const char*)(pc - o1) + o, scr);
      }
    }
    const u16* tw = ((l & 31) < 14 ? p.wg : p.wu) + (size_t)(lt * F_ + 896 * (pb & 1) + ((l & 31) < 14 ? (l & 31) : (l & 31) - 14) * 64);
    if (wv < 2) { if ((l & 31) < 28) dma4v(tw + (size_t)(32 * (TS0 + 2 * wv + (l >> 5))) * F_, scr); }
    else if (wv == 2) dma4(2u * ((512 * ((pb & 7) >> 1) + 16 * lt + (l >> 3)) * D_) + (l & 7) * 128, (const char*)p.u, scr);
    else dma4(2u * ((512 * ((pb & 7) >> 1) + 16 * lt + 8 + (l >> 3)) * D_) + (l & 7) * 128, (const char*)p.u, scr);
    const u32x4 ut0 = *(const u32x4*)(p.u + (size_t)(64 * lt + (t >> 3)) * D_ + 64 * dtile + 8 * (t & 7));
    const u32x4 ut1 = *(const u32x4*)(p.u + (size_t)(64 * lt + 32 + (t >> 3)) * D_ + 64 * dtile + 8 * (t & 7));
#pragma unroll
    for (int k = 0; k < 2; ++k) {
      const int i = wv + 4 * k, f = 7 * pb + i;
      if (i < 7) glds16(p.wd + (size_t)(16 * (f >> 4) + (l >> 2)) * D_ + 32 * (f & 15) + 8 * (l & 3), sm + PP_W + i * 1024);
    }
    const u32x4 gwv = *(const u32x4*)(p.gw + 8 * l);
    const u32x4 gx0 = *(const u32x4*)(p.u + (size_t)(8 * pb + 2 * wv) * D_ + 8 * l);
    const u32x4 gx1 = *(const u32x4*)(p.u + (size_t)(8 * pb + 2 * wv + 1) * D_ + 8 * l);
    {
      const u32x4 ones = {~0u, ~0u, ~0u, ~0u};
      st16wt(p.ws, (unsigned)WS_AT + (unsigned)(pb * 8192 + t * 16), ones);
      st16wt(p.ws, (unsigned)WS_AT + (unsigned)(pb * 8192 + 4096 + t * 16), ones);
    }
    float s0 = 0.f, s1 = 0.f;
#pragma unroll
    for (int e = 0; e < 4; ++e) {
      const float gl = bflo(gwv[e]), gh = bfhi(gwv[e]);
      unsigned p0 = pk_bf16(bflo(gx0[e]) * gl, bfhi(gx0[e]) * gh), p1 = pk_bf16(bflo(gx1[e]) * gl, bfhi(gx1[e]) * gh);
      s0 += bflo(p0); s0 += bfhi(p0); s1 += bflo(p1); s1 += bfhi(p1);
    }
    s0 = wsum63(s0); s1 = wsum63(s1);
    if (l == 63) {
      f32x2 gg = {bfr(sigmoidf_(bfr(s0))), bfr(sigmoidf_(bfr(s1)))};
      *(f32x2*)(p.ws + WS_G + (8 * pb + 2 * wv) * 4) = gg;
    }
#pragma unroll
    for (int e = 0; e < 4; ++e) {
      *(LDS unsigned*)(sm + PP_T + (t >> 3) * 132 + (t & 7) * 16 + 4 * e) = ut0[e];
      *(LDS unsigned*)(sm + PP_T + (32 + (t >> 3)) * 132 + (t & 7) * 16 + 4 * e) = ut1[e];
    }
    if (wv == 3) {
      for (int s = TS0 + 4; s < TS0 + 4 + TSPLIT; ++s) {
        if (l < 28) dma4v(tw + (size_t)(32 * s) * F_, scr);
        __builtin_amdgcn_s_sleep(TSLP);
      }
      asm volatile("s_waitcnt vmcnt(%0)" :: "n"(TSPLIT) : "memory");
    } else
    WAIT_V(0);
  }
  ST(p.ws, 224 + pb, 1);
  syncthreads();
  __builtin_amdgcn_s_setprio(PP2);
  if (w < 6) {
    const int c2 = w >= 3, T = w - 3 * c2;
    LDS char* pa = sm + PP_P + c2 * (41 * 528) + (16 * T + (l & 15)) * 528 + (l >> 4) * 16;
    LDS char* pq = sm + PP_Q + c2 * (16 * 528) + (l & 15) * 528 + (l >> 4) * 16;
    f32x4 acc0 = {0.f, 0.f, 0.f, 0.f}, acc1 = {0.f, 0.f, 0.f, 0.f};
#pragma unroll
    for (int s = 0; s < 4; ++s) {
      acc0 = __builtin_amdgcn_mfma_f32_16x16x32_f16(*(LDS f16x8*)(pa + s * 64), *(LDS f16x8*)(pq + s * 64), acc0, 0, 0, 0);
      acc1 = __builtin_amdgcn_mfma_f32_16x16x32_f16(*(LDS f16x8*)(pa + 256 + s * 64), *(LDS f16x8*)(pq + 256 + s * 64), acc1, 0, 0, 0);
    }
    const float is0 = *(LDS float*)(sm + PP_S + 8 * c2), is1 = *(LDS float*)(sm + PP_S + 8 * c2 + 4);
    LDS float* kc = (LDS float*)(sm + PP_KC) + c2 * 656;
    const int j = l & 15;
#pragma unroll
    for (int r = 0; r < 4; ++r) {
      const int row = 4 * (l >> 4) + r;
      int ci_;
      if (T == 0) ci_ = (row & 7) + 128 * (row >> 3) + 8 * j;
      else if (T == 1) ci_ = row < 8 ? 256 + (row & 3) + 64 * (row >> 2) + 4 * j : 384 + (row & 1) + 32 * ((row - 8) >> 1) + 2 * j;
      else ci_ = row < 8 ? 512 + 16 * row + j : ((row == 8 && j == 0) ? 640 : 641);
      kc[ci_] = acc0[r] * is0 + acc1[r] * is1;
    }
  }
  if (w >= 4) {
    const int t = tid - 256;
    if (w == TW2) {
      const unsigned scr = (unsigned)(size_t)sm + PP_S + 64;
      const u16* tw = ((l & 31) < 14 ? p.wg : p.wu) + (size_t)(lt * F_ + 896 * (pb & 1) + ((l & 31) < 14 ? (l & 31) : (l & 31) - 14) * 64);
      for (int s = TS0 + 4 + TSPLIT + (l >> 5); s < 16; s += 2) {
        if ((l & 31) < 28) dma4v(tw + (size_t)(32 * s) * F_, scr);
        __builtin_amdgcn_s_sleep(TSLP2);
      }
    }
    if (w >= 6) {
      const int h = l >> 5, blk = (l >> 4) & 1, q = (l & 15) >> 2, pp = l & 3;
#pragma unroll
      for (int k = 0; k < 4; ++k) {
        const int i = 4 * (w - 6) + k, f = 7 * pb + i;
        if (i < 7) {
          LDS char* b0 = sm + PP_W + i * 1024 + (4 * h + q) * 64 + (16 * blk + 4 * pp) * 2;
          s16x4 a = trread(b0), b = trread(b0 + 8 * 64);
          s16x8 r = __builtin_shufflevector(a, b, 0, 1, 2, 3, 4, 5, 6, 7);
          st16wt(p.ws, (unsigned)WS_WD + (unsigned)(((f & 15) * KS2 + (f >> 4)) * 1024 + l * 16), __builtin_bit_cast(u32x4, r));
        }
      }
    }
#pragma unroll
    for (int k = 0; k < 2; ++k) {
      const int dd = (t >> 3) + 32 * k, lc = (t & 7) * 8;
      u32x4 o4;
#pragma unroll
      for (int e = 0; e < 4; ++e) {
        u16 a = *(LDS u16*)(sm + PP_T + (lc + 2 * e) * 132 + 2 * dd), b = *(LDS u16*)(sm + PP_T + (lc + 2 * e + 1) * 132 + 2 * dd);
        o4[e] = (unsigned)a | ((unsigned)b << 16);
      }
      st16wt(p.ws, (unsigned)WS_UT + (unsigned)(((64 * dtile + dd) * L_ + 64 * lt + lc) * 2), o4);
    }
  }
  syncthreads();
  ST(p.ws, 224 + pb, 2);
  {
    const int c2 = w >> 2, c = tid & 255, k = 255 - c, cls = w & 3;
    LDS float* kc = (LDS float*)(sm + PP_KC) + c2 * 656;
    u32x4 ov;
    if (cls < 2) {
      const float lo = kc[384 + k], hi = kc[385 + k];
      const unsigned hh = pk_bf16(hi, hi), ll = pk_bf16(lo, lo);
      ov[0] = hh; ov[1] = (k & 1) ? hh : ((hh & 0xffffu) | (ll & 0xffff0000u)); ov[2] = ll; ov[3] = ll;
    } else if (cls == 2) {
      const float x0 = kc[256 + 2 * k], x1 = kc[257 + 2 * k], x2 = kc[258 + 2 * k];
      ov[0] = pk_bf16(x2, x2); ov[1] = pk_bf16(x1, x1); ov[2] = pk_bf16(x1, x0); ov[3] = pk_bf16(x0, x0);
    } else {
      const bool c_ = l < 32;
      LDS float* b = kc + (c_ ? 128 + 4 * k : 8 * k);
      const float v0 = b[c_ ? 4 : 7], v1 = b[c_ ? 3 : 6], v2 = b[c_ ? 2 : 5], v3 = b[c_ ? 2 : 4];
      const float v4 = b[c_ ? 2 : 3], v5 = b[c_ ? 1 : 2], v6 = b[c_ ? 0 : 1], v7 = b[0];
      ov[0] = pk_bf16(v0, v1); ov[1] = pk_bf16(v2, v3); ov[2] = pk_bf16(v4, v5); ov[3] = pk_bf16(v6, v7);
    }
    st16wt(p.ws, (unsigned)WS_KR + (unsigned)(((2 * pb + c2) * KROW + 8 * c) * 2), ov);
    if (tid < 8) { u32x4 z = {0u, 0u, 0u, 0u}; st16wt(p.ws, (unsigned)WS_KR + (unsigned)(((2 * pb + (tid >> 2)) * KROW + 2048 + 8 * (tid & 3)) * 2), z); }
  }
  WAIT_V(0);
  ST(p.ws, 224 + pb, 3);
}

__global__ void __launch_bounds__(512, 4) k1(P1 p) {
  extern __shared__ __attribute__((aligned(16))) char smem[];
  LDS char* sm = (LDS char*)smem;
  if (blockIdx.x < 224) gemm1(p, sm, blockIdx.x); else prep(p, sm, blockIdx.x - 224);
}

struct P2 { const u16* dskip; u16* out; char* ws; };
constexpr int CV_K = 0;
constexpr int CV_U = 2 * 4 * 4160;
constexpr int CV_R = CV_U + 2 * 4608;
constexpr int K2_LDS = 65536 + 8192;

__device__ __forceinline__ void conv(const P2& p, LDS char* sm, int cb) {
  const int tid = threadIdx.x, w = __builtin_amdgcn_readfirstlane(tid >> 6), l = tid & 63;
  ST(p.ws, 512 + cb, 0);
  __builtin_amdgcn_s_setprio(3);
  const u16* ut = (const u16*)(p.ws + WS_UT) + (size_t)(2 * cb) * L_;
  const u16* kr = (const u16*)(p.ws + WS_KR) + (size_t)(2 * cb) * KROW;
  u32x4 uv[2];
#pragma unroll
  for (int c = 0; c < 2; ++c) uv[c] = *(const u32x4*)(ut + c * L_ + 8 * tid);
  u32x4 k0[3]; u32x2 k1v[3];
#pragma unroll
  for (int i = 0; i < 3; ++i) {
    int job = tid + 256 * i; job = job < 520 ? job : 519;
    const int c2 = job >= 260, c = job - 260 * c2;
    k0[i] = *(const u32x4*)(kr + c2 * KROW + 8 * c);
    k1v[i] = *(const u32x2*)(kr + c2 * KROW + 8 * c + 8);
    if (c == 259) { k1v[i][0] = 0u; k1v[i][1] = 0u; }
  }
  const float dsk = bf2f(p.dskip[2 * cb + (w >> 1)]);
#pragma unroll
  for (int c = 0; c < 2; ++c) {
    *(LDS u32x4*)(sm + CV_U + c * 4608 + 512 + 16 * tid) = uv[c];
    if (tid < 32) { u32x4 z = {0u, 0u, 0u, 0u}; *(LDS u32x4*)(sm + CV_U + c * 4608 + 16 * tid) = z; }
  }
#pragma unroll
  for (int i = 0; i < 3; ++i) {
    const int job = tid + 256 * i;
    if (job < 520) {
      const int c2 = job >= 260, c = job - 260 * c2;
      unsigned dd[6] = {k0[i][0], k0[i][1], k0[i][2], k0[i][3], k1v[i][0], k1v[i][1]};
      LDS char* b = sm + CV_K + c2 * (4 * 4160) + 16 * c;
      u32x4 c0 = {dd[0], dd[1], dd[2], dd[3]}, c2v = {dd[1], dd[2], dd[3], dd[4]}, c1, c3;
#pragma unroll
      for (int e = 0; e < 4; ++e) { c1[e] = __builtin_amdgcn_alignbit(dd[e + 1], dd[e], 16); c3[e] = __builtin_amdgcn_alignbit(dd[e + 2], dd[e + 1], 16); }
      *(LDS u32x4*)(b) = c0; *(LDS u32x4*)(b + 4160) = c1; *(LDS u32x4*)(b + 2 * 4160) = c2v; *(LDS u32x4*)(b + 3 * 4160) = c3;
    }
  }
  syncthreads();
  ST(p.ws, 512 + cb, 1);
  const int ch = w >> 1, rh = w & 1, j = l & 15, g = l >> 4;
  const int e = 3 - (j & 3);
  LDS char* ka = sm + CV_K + ch * (4 * 4160) + e * 4160 + 2 * (2031 - j + 8 * g - e);
  LDS char* ub = sm + CV_U + ch * 4608 + 2 * (240 + 16 * j + 8 * g);
  f32x4 acc[8];
#pragma unroll
  for (int c = 0; c < 8; ++c) acc[c] = f32x4{0.f, 0.f, 0.f, 0.f};
#pragma unroll
  for (int rr = 0; rr < 4; ++rr) {
    const int r = 4 * rh + rr;
    bf16x8 bf[8];
#pragma unroll
    for (int t = 0; t < 8; ++t) bf[t] = *(LDS bf16x8*)(ub + 512 * t - 64 * CVR);
#pragma unroll
    for (int q = 0; q < 8; ++q) {
      LDS char* a = ka - 64 * (8 * CVQ + CVR);
      s16x4 lo = *(LDS s16x4*)a, hi = *(LDS s16x4*)(a + 8);
      bf16x8 af = cat8(lo, hi);
#pragma unroll
      for (int c = q; c < 8; ++c) acc[c] = __builtin_amdgcn_mfma_f32_16x16x32_bf16(af, bf[c - q], acc[c], 0, 0, 0);
    }
  }
  ST(p.ws, 512 + cb, 2);
  LDS char* rb = sm + CV_R;
#pragma unroll
  for (int c = 0; c < 4; ++c) *(LDS f32x4*)(rb + (w * 4 + c) * 1024 + l * 16) = acc[4 * (rh ^ 1) + c];
  syncthreads();
  u16* at = (u16*)(p.ws + WS_AT) + (size_t)(2 * cb + ch) * L_;
#pragma unroll
  for (int c = 0; c < 4; ++c) {
    f32x4 o = *(LDS f32x4*)(rb + ((w ^ 1) * 4 + c) * 1024 + l * 16);
    f32x4 y = acc[4 * rh + c] + o;
    const int lpos = 256 * (4 * rh + c) + 16 * j + 4 * g;
    u32x2 uu = *(LDS u32x2*)(sm + CV_U + ch * 4608 + 512 + 2 * lpos);
    unsigned y0 = pk_bf16(y[0], y[1]), y1 = pk_bf16(y[2], y[3]);
    unsigned s0 = pk_bf16(bflo(uu[0]) * dsk, bfhi(uu[0]) * dsk), s1 = pk_bf16(bflo(uu[1]) * dsk, bfhi(uu[1]) * dsk);
    unsigned a0 = pk_bf16(bflo(y0) + bflo(s0), bfhi(y0) + bfhi(s0)), a1 = pk_bf16(bflo(y1) + bflo(s1), bfhi(y1) + bfhi(s1));
    unsigned long long v = (unsigned long long)a0 | ((unsigned long long)a1 << 32);
    if (v == ~0ull) v = 0x7FC07FC07FC07FC0ull;
    __hip_atomic_store((GLB unsigned long long*)(at + lpos), v, __ATOMIC_RELAXED, __HIP_MEMORY_SCOPE_AGENT);
  }
  ST(p.ws, 512 + cb, 3);
}

#define PFX 9
constexpr int PF = PFX;
#define APRE 4
struct F2 { bf16x8 a0, a1, b0, b1; };
#define GLD16(dst, voff, sbase, off) asm volatile("global_load_dwordx4 %0, %1, %2 offset:%3" : "=v"(dst) : "v"(voff), "s"(sbase), "i"(off))
template <int S> __device__ __forceinline__ void g2_ld(F2& f, unsigned vo, const char* A0, const char* B0) {
  const char *a = A0 + (S >> 2) * 4096, *b = B0 + (S >> 2) * 4096;
  GLD16(f.a0, vo, a, (S & 3) * 1024); GLD16(f.b0, vo, b, (S & 3) * 1024);
  GLD16(f.a1, vo, a + KS2 * 1024, (S & 3) * 1024); GLD16(f.b1, vo, b + KS2 * 1024, (S & 3) * 1024); }
template <int S> __device__ __forceinline__ void g2_pro(F2 (&f)[PF], unsigned vo, const char* A0, const char* B0) {
  g2_ld<S>(f[S], vo, A0, B0);
  if constexpr (S + 1 < PF) g2_pro<S + 1>(f, vo, A0, B0); }
template <int S> __device__ __forceinline__ void g2_step(F2 (&f)[PF], unsigned vo, const char* A0, const char* B0, f32x16 (&acc)[2][2],
                                                         unsigned va, const char* AT, unsigned long long (&av8)[4]) {
  F2& c = f[S % PF];
  constexpr int pend = (S + PF <= 28 ? PF : 28 - S) - 1;
  asm volatile("s_waitcnt vmcnt(%4)" : "+v"(c.a0), "+v"(c.a1), "+v"(c.b0), "+v"(c.b1) : "i"(4 * pend + (S + PF > 27 + APRE ? 4 : 0)) : "memory");
  acc[0][0] = MFMA32(c.a0, c.b0, acc[0][0]); acc[0][1] = MFMA32(c.a0, c.b1, acc[0][1]);
  acc[1][0] = MFMA32(c.a1, c.b0, acc[1][0]); acc[1][1] = MFMA32(c.a1, c.b1, acc[1][1]);
  SCHED();
  if constexpr (S + PF < 28) g2_ld<S + PF>(c, vo, A0, B0);
  if constexpr (S + PF == 27 + APRE) {
#pragma unroll
    for (int i = 0; i < 4; ++i)
      asm volatile("global_load_dwordx2 %0, %1, %2 sc1" : "=v"(av8[i]) : "v"(va), "s"(AT + (size_t)i * 16 * L_ * 2) : "memory");
  }
  SCHED();
  if constexpr (S + 1 < 28) g2_step<S + 1>(f, vo, A0, B0, acc, va, AT, av8);
}
__device__ __forceinline__ void gemm2(const P2& p, LDS char* sm, int b) {
  const int tid = threadIdx.x, w = __builtin_amdgcn_readfirstlane(tid >> 6), l = tid & 63;
  const int xcd = b & 7, idx = b >> 3;
  const int mt = 4 * xcd + (idx & 3), nt = idx >> 2;
#define K2NT nt
#define K2MT mt
  const char* A0 = p.ws + WS_WD + ((size_t)(2 * (K2NT)) * KS2 + 28 * w) * 1024;
  const char* B0 = p.ws + WS_H + ((size_t)(2 * (K2MT)) * KS2 + 28 * w) * 1024;
  f32x16 acc[2][2];
#pragma unroll
  for (int i = 0; i < 2; ++i)
#pragma unroll
    for (int j = 0; j < 2; ++j)
#pragma unroll
      for (int r = 0; r < 16; ++r) acc[i][j][r] = 0.f;
  ST(p.ws, 768 + b, 0);
  __builtin_amdgcn_s_setprio(G2P);
  const int tl = 16 * w + (l & 15), tok = 64 * mt + tl;
  float g;
  asm volatile("global_load_dword %0, %1, %2" : "=v"(g) : "v"(tok * 4), "s"(p.ws + WS_G));
  F2 f[PF];
  g2_pro<0>(f, l * 16, A0, B0);
  SCHED();
  unsigned long long av8[4];
  g2_step<0>(f, l * 16, A0, B0, acc, (unsigned)(((tid >> 4) * L_ + 4 * (tid & 15)) * 2),
             p.ws + WS_AT + ((size_t)(64 * nt) * L_ + 64 * mt) * 2, av8);
  asm volatile("s_waitcnt vmcnt(0)" : "+v"(g), "+v"(av8[0]), "+v"(av8[1]), "+v"(av8[2]), "+v"(av8[3]) :: "memory");
  ST(p.ws, 768 + b, 1);
  GLB unsigned long long* at = (GLB unsigned long long*)((const u16*)(p.ws + WS_AT) + (size_t)(64 * nt + (tid >> 4)) * L_ + 64 * mt + 4 * (tid & 15));
#pragma unroll
  for (int i = 0; i < 2; ++i)
#pragma unroll
    for (int j = 0; j < 2; ++j)
#pragma unroll
      for (int q = 0; q < 4; ++q) {
        f32x4 v = {acc[i][j][4 * q], acc[i][j][4 * q + 1], acc[i][j][4 * q + 2], acc[i][j][4 * q + 3]};
        *(LDS f32x4*)(sm + w * 16384 + ((2 * i + j) * 4 + q) * 1024 + l * 16) = v;
      }
  for (unsigned spins = 0; spins < (1u << 22); ++spins) {
    const bool bad = av8[0] == ~0ull || av8[1] == ~0ull || av8[2] == ~0ull || av8[3] == ~0ull;
    if (__builtin_amdgcn_ballot_w64(bad) == 0ull) break;
    __builtin_amdgcn_s_sleep(1);
#pragma unroll
    for (int i = 0; i < 4; ++i) av8[i] = __hip_atomic_load(at + (size_t)i * 16 * L_ / 4, __ATOMIC_RELAXED, __HIP_MEMORY_SCOPE_AGENT);
  }
#pragma unroll
  for (int i = 0; i < 4; ++i) *(LDS unsigned long long*)(sm + 65536 + ((tid >> 4) + 16 * i) * 128 + (tid & 15) * 8) = av8[i];
  syncthreads();
  ST(p.ws, 768 + b, 2);
  const float og = bfr(1.f - g);
  const int q = l >> 4, qq = (l & 15) >> 2, pp = l & 3;
#pragma unroll
  for (int k = 0; k < 2; ++k) {
    unsigned ov[4];
#pragma unroll
    for (int h = 0; h < 2; ++h) {
      LDS char* rp = sm + ((2 * k + (w >> 1)) * 4 + q) * 1024 + (16 * (w & 1) + (l & 15) + 32 * h) * 16;
      f32x4 s = *(LDS f32x4*)rp;
#pragma unroll
      for (int ww = 1; ww < 4; ++ww) s += *(LDS f32x4*)(rp + ww * 16384);
      s16x4 av = trread(sm + 65536 + (32 * k + 8 * q + 4 * h + qq) * 128 + (16 * w + 4 * pp) * 2);
      float o[4];
#pragma unroll
      for (int e = 0; e < 4; ++e) o[e] = bfr(g * bf2f((u16)av[e])) + bfr(og * bfr(s[e]));
      ov[2 * h] = pk_bf16(o[0], o[1]); ov[2 * h + 1] = pk_bf16(o[2], o[3]);
    }
    u32x4 o4 = {ov[0], ov[1], ov[2], ov[3]};
    *(u32x4*)(p.out + (size_t)tok * D_ + 64 * nt + 32 * k + 8 * q) = o4;
  }
  ST(p.ws, 768 + b, 3);
}

__global__ void __launch_bounds__(256, 2) k2(P2 p) {
  extern __shared__ __attribute__((aligned(16))) char smem[];
  LDS char* sm = (LDS char*)smem;
  if (blockIdx.x < 256) conv(p, sm, blockIdx.x); else gemm2(p, sm, blockIdx.x - 256);
}

#if defined(__HIP_DEVICE_COMPILE__)
#pragma clang attribute pop
#endif
extern "C" void kernel_launch(void* const* d_in, const int* in_sizes, int n_in, void* d_out, int out_size,
                              void* d_ws, size_t ws_size, hipStream_t stream) {
  P1 a;
  a.u = (const u16*)d_in[0]; a.are = (const u16*)d_in[1]; a.aim = (const u16*)d_in[2]; a.bre = (const u16*)d_in[3];
  a.bim = (const u16*)d_in[4]; a.cre = (const u16*)d_in[5]; a.cim = (const u16*)d_in[6]; a.logdt = (const u16*)d_in[7];
  a.wg = (const u16*)d_in[10]; a.wu = (const u16*)d_in[11]; a.wd = (const u16*)d_in[12];
  a.gw = (const u16*)d_in[13]; a.ws = (char*)d_ws;
  P2 b; b.dskip = (const u16*)d_in[8]; b.out = (u16*)d_out; b.ws = (char*)d_ws;
  (void)hipFuncSetAttribute((const void*)k1, hipFuncAttributeMaxDynamicSharedMemorySize, 81920);
  (void)hipFuncSetAttribute((const void*)k2, hipFuncAttributeMaxDynamicSharedMemorySize, K2_LDS);
  hipLaunchKernelGGL(k1, dim3(480), dim3(512), 81920, stream, a);
  hipLaunchKernelGGL(k2, dim3(512), dim3(256), K2_LDS, stream, b);
}
```

```cpp
#include <hip/hip_runtime.h>
#include <hip/hip_bf16.h>
#include <stdint.h>

#if defined(__HIP_DEVICE_COMPILE__)
#pragma clang attribute push(__attribute__((target("no-packed-fp32-ops"))), apply_to = function)
#endif
typedef unsigned short u16;
typedef __attribute__((ext_vector_type(4))) short s16x4;
typedef __attribute__((ext_vector_type(8))) short s16x8;
typedef __attribute__((ext_vector_type(8))) __bf16 bf16x8;
typedef __attribute__((ext_vector_type(2))) __bf16 bf16x2;
typedef __attribute__((ext_vector_type(8))) _Float16 f16x8;
typedef __attribute__((ext_vector_type(2))) _Float16 f16x2;
typedef __attribute__((ext_vector_type(16))) float f32x16;
typedef __attribute__((ext_vector_type(4))) float f32x4;
typedef __attribute__((ext_vector_type(2))) float f32x2;
typedef __attribute__((ext_vector_type(4))) unsigned u32x4;
typedef __attribute__((ext_vector_type(2))) unsigned u32x2;
#define LDS __attribute__((address_space(3)))
#define GLB __attribute__((address_space(1)))

constexpr int L_ = 2048, D_ = 512, N_ = 128, F_ = 1792;
constexpr int KS2 = F_ / 16;
constexpr int KROW = 2080;
constexpr size_t WS_H    = 0;
constexpr size_t WS_WD   = WS_H + (size_t)64 * KS2 * 1024;
constexpr size_t WS_UT   = WS_WD + (size_t)16 * KS2 * 1024;
constexpr size_t WS_KR   = WS_UT + (size_t)D_ * L_ * 2;
constexpr size_t WS_AT   = WS_KR + (size_t)D_ * KROW * 2 + 4096;
constexpr size_t WS_G    = WS_AT + (size_t)D_ * L_ * 2;
constexpr unsigned WS_BYTES = (unsigned)(WS_G + (size_t)L_ * 4);

__device__ __forceinline__ float u2f(unsigned v) { return __builtin_bit_cast(float, v); }
__device__ __forceinline__ unsigned f2u(float v) { return __builtin_bit_cast(unsigned, v); }
__device__ __forceinline__ void syncthreads() {
  __builtin_amdgcn_fence(__ATOMIC_RELEASE, "workgroup"); __builtin_amdgcn_s_barrier(); __builtin_amdgcn_fence(__ATOMIC_ACQUIRE, "workgroup"); }
__device__ __forceinline__ float shx(float v, int lane, int m) {
  return u2f((unsigned)__builtin_amdgcn_ds_bpermute((lane ^ m) << 2, (int)f2u(v))); }
__device__ __forceinline__ float bf2f(u16 v) { return u2f((unsigned)v << 16); }
__device__ __forceinline__ unsigned pk_bf16(float a, float b) {
  f32x2 t = {a, b}; bf16x2 r = __builtin_convertvector(t, bf16x2); return __builtin_bit_cast(unsigned, r); }
__device__ __forceinline__ float bflo(unsigned p) { return u2f(p << 16); }
__device__ __forceinline__ float bfhi(unsigned p) { return u2f(p & 0xffff0000u); }
__device__ __forceinline__ float bfr(float a) { return bflo(pk_bf16(a, 0.f)); }
__device__ __forceinline__ unsigned pk_f16(float a, float b) {
  f32x2 t = {a, b}; f16x2 r = __builtin_convertvector(t, f16x2); return __builtin_bit_cast(unsigned, r); }
__device__ __forceinline__ float sigmoidf_(float x) { return __builtin_amdgcn_rcpf(1.f + __builtin_amdgcn_exp2f(-1.44269504089f * x)); }
#define WAIT_V(n) asm volatile("s_waitcnt vmcnt(" #n ")" ::: "memory")
#define WAIT_L(n) asm volatile("s_waitcnt lgkmcnt(" #n ")" ::: "memory")
#define BAR() __builtin_amdgcn_s_barrier()
#define SCHED() __builtin_amdgcn_sched_barrier(0)
#define ST(ws, row, i)
__device__ __forceinline__ void glds16(const void* g, LDS char* l) {
  __builtin_amdgcn_global_load_lds((const GLB void*)g, (LDS void*)l, 16, 0, 0); }

__device__ __forceinline__ void st16wt(char* ws, unsigned off, u32x4 v) {
  __builtin_amdgcn_raw_buffer_store_b128(v, __builtin_amdgcn_make_buffer_rsrc(ws, 0, WS_BYTES, 0x00020000), off, 0, 16); }
__device__ __forceinline__ s16x4 trread(LDS char* p) { return __builtin_amdgcn_ds_read_tr16_b64_v4i16((LDS s16x4*)p); }
__device__ __forceinline__ bf16x8 cat8(s16x4 a, s16x4 b) {
  s16x8 r = __builtin_shufflevector(a, b, 0, 1, 2, 3, 4, 5, 6, 7); return __builtin_bit_cast(bf16x8, r); }

struct P1 {
  const u16 *u, *are, *aim, *bre, *bim, *cre, *cim, *logdt, *wg, *wu, *wd, *gw;
  char* ws;
};

#define DSR_TR(dst, addr, off) asm volatile("ds_read_b64_tr_b16 %0, %1 offset:%2" : "=v"(dst) : "v"(addr), "i"(off))
#define DSR_128(dst, addr, off) asm volatile("ds_read_b128 %0, %1 offset:%2" : "=v"(dst) : "v"(addr), "i"(off))
__device__ __forceinline__ void dma16(unsigned voff, const char* sbase, unsigned ldsb) {
  asm volatile("s_mov_b32 m0, %2\n\ts_nop 0\n\tglobal_load_lds_dwordx4 %0, %1" :: "v"(voff), "s"(sbase), "s"(ldsb) : "memory"); }
__device__ __forceinline__ void dma4(unsigned voff, const char* sbase, unsigned ldsb) {
  asm volatile("s_mov_b32 m0, %2\n\ts_nop 0\n\tglobal_load_lds_dword %0, %1" :: "v"(voff), "s"(sbase), "s"(ldsb) : "memory"); }
struct Frag { s16x4 a0l, a0h, a1l, a1h; bf16x8 b0, b1; };
#define FRAG_WAIT(f) asm volatile("s_waitcnt lgkmcnt(0)" : "+v"(f.a0l), "+v"(f.a0h), "+v"(f.a1l), "+v"(f.a1h), "+v"(f.b0), "+v"(f.b1) :: "memory")
#define MFMA32(a, b, c) __builtin_amdgcn_mfma_f32_32x32x16_bf16(a, b, c, 0, 0, 0)
struct G1 {
  unsigned a0, a1, bx[4];
  unsigned vg, vu, vx0, vx1;
  const char *wg, *wu, *x;
  unsigned lw, lx;
};
template <int S, int KS> __device__ __forceinline__ void g1_rd0(const G1& g, Frag& f) {
  constexpr int wo = (S % 3) * 16384 + KS * 4096, xo = ((S >> 1) & 1) * 16384;
  DSR_TR(f.a0l, g.a0, wo); DSR_TR(f.a0h, g.a0, wo + 1024); DSR_128(f.b0, g.bx[2 * (S & 1) + KS], xo); }
template <int S, int KS> __device__ __forceinline__ void g1_rd1(const G1& g, Frag& f) {
  constexpr int wo = (S % 3) * 16384 + KS * 4096, xo = ((S >> 1) & 1) * 16384;
  DSR_128(f.b1, g.bx[2 * (S & 1) + KS], xo + 4096); DSR_TR(f.a1l, g.a1, wo); DSR_TR(f.a1h, g.a1, wo + 1024); }
template <int S> __device__ __forceinline__ void g1_issueW(const G1& g) {
  dma16(g.vg, g.wg + (size_t)S * 32 * F_ * 2, g.lw + (S % 3) * 16384);
  dma16(g.vu, g.wu + (size_t)S * 32 * F_ * 2, g.lw + (S % 3) * 16384 + 8192); }
template <int J> __device__ __forceinline__ void g1_issueX(const G1& g) {
  dma16(g.vx0, g.x + J * 128, g.lx + (J & 1) * 16384);
  dma16(g.vx1, g.x + J * 128, g.lx + (J & 1) * 16384 + 1024); }
template <int S> __device__ __forceinline__ void g1_stage(const G1& g, Frag& f0, Frag& f1, f32x16 (&acc)[2][2]) {
  FRAG_WAIT(f0);
  {
    const bf16x8 a0 = cat8(f0.a0l, f0.a0h), a1 = cat8(f0.a1l, f0.a1h);
    acc[0][0] = MFMA32(a0, f0.b0, acc[0][0]); SCHED();
    g1_rd0<S, 1>(g, f1); SCHED();
    acc[0][1] = MFMA32(a0, f0.b1, acc[0][1]); SCHED();
    g1_rd1<S, 1>(g, f1); SCHED();
    acc[1][0] = MFMA32(a1, f0.b0, acc[1][0]); SCHED();
    acc[1][1] = MFMA32(a1, f0.b1, acc[1][1]); SCHED();
  }
  FRAG_WAIT(f1);
  if constexpr (S < 15) {
    if constexpr (S >= 14) { WAIT_V(0); } else if constexpr ((S & 1) == 0) { WAIT_V(4); } else { WAIT_V(2); }
    BAR();
  }
  {
    const bf16x8 a0 = cat8(f1.a0l, f1.a0h), a1 = cat8(f1.a1l, f1.a1h);
    acc[0][0] = MFMA32(a0, f1.b0, acc[0][0]); SCHED();
    if constexpr (S < 15) g1_rd0<S + 1, 0>(g, f0);
    SCHED();
    acc[0][1] = MFMA32(a0, f1.b1, acc[0][1]); SCHED();
    if constexpr (S < 15) g1_rd1<S + 1, 0>(g, f0);
    SCHED();
    acc[1][0] = MFMA32(a1, f1.b0, acc[1][0]); SCHED();
    if constexpr (S + 3 < 16) g1_issueW<S + 3>(g);
    SCHED();
    acc[1][1] = MFMA32(a1, f1.b1, acc[1][1]); SCHED();
    if constexpr ((S & 1) == 1 && (S >> 1) + 2 < 8) g1_issueX<(S >> 1) + 2>(g);
    SCHED();
  }
  if constexpr (S < 15) g1_stage<S + 1>(g, f0, f1, acc);
}
__device__ __forceinline__ void gemm1(const P1& p, LDS char* sm, int b) {
  const int tid = threadIdx.x, w = __builtin_amdgcn_readfirstlane(tid >> 6), l = tid & 63;
  const int xcd = b & 7, idx = b >> 3;
  const int mt = 4 * (xcd >> 1) + (idx & 3), nt = 7 * (xcd & 1) + (idx >> 2);
  const int wm = w & 1, wn = w >> 1;
  const unsigned smb = (unsigned)(size_t)sm;
  G1 g;
  {
    const int krow = 4 * w + (l >> 4);
    g.vg = 2u * (krow * F_ + 8 * ((l & 15) ^ (2 * (l >> 4))));
    g.vu = 2u * (krow * F_ + 8 * ((l & 15) ^ (2 * ((l >> 4) | 4))));
    const int xr0 = 16 * w + (l >> 3);
    g.vx0 = 2u * (xr0 * D_ + 8 * ((l & 7) ^ ((xr0 >> 1) & 7)));
    g.vx1 = 2u * ((xr0 + 8) * D_ + 8 * ((l & 7) ^ (((xr0 + 8) >> 1) & 7)));
    g.wg = (const char*)(p.wg + nt * 128); g.wu = (const char*)(p.wu + nt * 128); g.x = (const char*)(p.u + (size_t)mt * 128 * D_);
    g.lw = smb + w * 1024; g.lx = smb + 49152 + w * 2048;
    const int h = l >> 5, blk = (l >> 4) & 1, q = (l & 15) >> 2, pp = l & 3, img = pp >> 1;
    g.a0 = smb + img * 8192 + (8 * h + q) * 256 + 32 * ((2 * wn) ^ (q | (img << 2))) + 16 * blk + 8 * (pp & 1);
    g.a1 = g.a0 ^ 32;
    const int xrow = 64 * wm + (l & 31);
    const unsigned bb = 49152 + xrow * 128 + 16 * (h ^ ((xrow >> 1) & 7));
#pragma unroll
    for (int c = 0; c < 4; ++c) g.bx[c] = smb + (bb ^ (c * 32));
  }
  f32x16 acc[2][2];
#pragma unroll
  for (int i = 0; i < 2; ++i)
#pragma unroll
    for (int j = 0; j < 2; ++j)
#pragma unroll
      for (int r = 0; r < 16; ++r) acc[i][j][r] = 0.f;
  Frag f0, f1;
  ST(p.ws, b, 0);
#define G1PRIO 2
  __builtin_amdgcn_s_setprio(G1PRIO);
  g1_issueX<0>(g); g1_issueW<0>(g); g1_issueW<1>(g); g1_issueX<1>(g); g1_issueW<2>(g);
  WAIT_V(6); BAR();
  ST(p.ws, b, 1);
  g1_rd0<0, 0>(g, f0); g1_rd1<0, 0>(g, f0);
  g1_stage<0>(g, f0, f1, acc);
  ST(p.ws, b, 2);
#pragma unroll
  for (int i = 0; i < 2; ++i)
#pragma unroll
    for (int j = 0; j < 2; ++j) {
      unsigned o[4];
#pragma unroll
      for (int hh = 0; hh < 2; ++hh) {
        float hv[4];
#pragma unroll
        for (int e = 0; e < 4; e += 2) {
          unsigned gp = pk_bf16(acc[i][j][8 * hh + e], acc[i][j][8 * hh + e + 1]);
          unsigned up = pk_bf16(acc[i][j][8 * hh + 4 + e], acc[i][j][8 * hh + 4 + e + 1]);
          float g0 = bflo(gp), g1 = bfhi(gp);
          unsigned sp = pk_bf16(g0 * sigmoidf_(g0), g1 * sigmoidf_(g1));
          hv[e] = bflo(sp) * bflo(up); hv[e + 1] = bfhi(sp) * bfhi(up);
        }
        o[2 * hh] = pk_bf16(hv[0], hv[1]); o[2 * hh + 1] = pk_bf16(hv[2], hv[3]);
      }
      const int tb = 4 * mt + 2 * wm + j, ks = 8 * nt + 2 * wn + i;
      u32x4 ov = {o[0], o[1], o[2], o[3]};
      st16wt(p.ws, (unsigned)WS_H + (unsigned)((tb * KS2 + ks) * 1024 + l * 16), ov);
    }
  ST(p.ws, b, 3);
}

constexpr int PP_P = 0;
constexpr int PP_Q = PP_P + 2 * 41 * 528;
constexpr int PP_KC = PP_Q + 2 * 16 * 528;
constexpr int PP_T = PP_KC + 2 * 656 * 4;
constexpr int PP_W = PP_T + 64 * 132;
constexpr int PP_S = PP_W + 7 * 1024;
static_assert(PP_S + 64 + 256 <= 81920, "lds");
static_assert(PP_W % 16 == 0 && PP_KC % 16 == 0 && PP_T % 16 == 0, "al");

__device__ __forceinline__ void cmul(float ar, float ai, float br, float bi, float& r, float& i) {
  r = ar * br - ai * bi; i = ar * bi + ai * br; }

#define TS0 3
#define TSLP 9
#define TSPLIT 3
#define TSLP2 4
#define PP1 3
#define PP2 3
#define CVP 3
#define G2P 0
#define CVR r
#define CVQ q
#define CVM 0
#define CT 1
#define CTB 256
#define CTK2 9216u
#define OWT 0
#define KDPP 0
#define FASTT 1
#define TF0 13
#define TFSLP 10
#define TW2 7
template <int CTRL, int RM> __device__ __forceinline__ float dppf(float v) {
  return u2f((unsigned)__builtin_amdgcn_update_dpp(0, (int)f2u(v), CTRL, RM, 0xf, true)); }
__device__ __forceinline__ float wsum63(float v) {
  v += dppf<0x111, 0xf>(v); v += dppf<0x112, 0xf>(v); v += dppf<0x114, 0xf>(v); v += dppf<0x118, 0xf>(v);
  v += dppf<0x142, 0xa>(v); v += dppf<0x143, 0xc>(v); return v; }
__device__ __forceinline__ float wmax63(float v) {
  v = fmaxf(v, dppf<0x111, 0xf>(v)); v = fmaxf(v, dppf<0x112, 0xf>(v)); v = fmaxf(v, dppf<0x114, 0xf>(v)); v = fmaxf(v, dppf<0x118, 0xf>(v));
  v = fmaxf(v, dppf<0x142, 0xa>(v)); v = fmaxf(v, dppf<0x143, 0xc>(v)); return v; }
__device__ __forceinline__ void dma4v(const void* g, unsigned ldsb) {
  asm volatile("s_mov_b32 m0, %1\n\ts_nop 0\n\tglobal_load_lds_dword %0, off" :: "v"(g), "s"(ldsb) : "memory"); }

__device__ __forceinline__ void prep(const P1& p, LDS char* sm, int pb) {
  const int tid = threadIdx.x, w = __builtin_amdgcn_readfirstlane(tid >> 6), l = tid & 63;
  const int lt = pb >> 3, dtile = pb & 7;
  ST(p.ws, 224 + pb, 0);
  if (w < 4) {
    __builtin_amdgcn_s_setprio(PP1);
    const int ch = w >> 1, n = tid & 127, d = 2 * pb + ch, o = d * N_ + n;
    const float are = bf2f(p.are[o]), aim = bf2f(p.aim[o]);
    const float bre = bf2f(p.bre[o]), bim = bf2f(p.bim[o]), cre = bf2f(p.cre[o]), cim = bf2f(p.cim[o]);
    const float dt = bfr(expf(bf2f(p.logdt[d])));
    const float zr = dt * are, zi = dt * aim;
    float wr, wi;
    cmul(cre, cim, dt * bre, dt * bim, wr, wi);
    const float m = wmax63(fmaxf(fabsf(wr), fabsf(wi)));
    int ex = (int)((unsigned)__builtin_amdgcn_readlane((int)f2u(m), 63) >> 23) & 255;
    ex = ex < 2 ? 2 : (ex > 250 ? 250 : ex);
    const float sc = u2f((unsigned)(253 - ex) << 23), isc = u2f((unsigned)(ex + 1) << 23);
    wr *= sc; wi *= sc;
    if (l == 0) *(LDS float*)(sm + PP_S + 4 * w) = isc;
    const float zl = zr * 1.44269504089f, rv = zi * 0.15915494309f;
    auto E = [&](float x, float& er, float& ei) {
      float mag = __builtin_amdgcn_exp2f(zl * x), ph = __builtin_amdgcn_fractf(rv * x);
      er = mag * __builtin_amdgcn_cosf(ph); ei = mag * __builtin_amdgcn_sinf(ph); };
    LDS char* Pb = sm + PP_P + ch * (41 * 528) + n * 4;
    LDS char* Qb = sm + PP_Q + ch * (16 * 528) + n * 4;
    auto stP = [&](int row, float r, float i) { *(LDS unsigned*)(Pb + row * 528) = pk_f16(r, i); };
    auto stQ = [&](int row, float r, float i) { *(LDS unsigned*)(Qb + row * 528) = pk_f16(r, -i); };
    {
      float qr[16], qi[16];
      qr[0] = 1.f; qi[0] = 0.f; E(8.f, qr[1], qi[1]);
#pragma unroll
      for (int j = 2; j < 16; ++j) cmul(qr[j / 2], qi[j / 2], qr[j - j / 2], qi[j - j / 2], qr[j], qi[j]);
#pragma unroll
      for (int j = 0; j < 16; ++j) stQ(j, qr[j], qi[j]);
    }
    float e128r, e128i, e256r, e256i, e512r, e512i, e1kr, e1ki, e2kr, e2ki;
    E(128.f, e128r, e128i); E(256.f, e256r, e256i); E(512.f, e512r, e512i); E(1024.f, e1kr, e1ki); E(2048.f, e2kr, e2ki);
    float ar[16], ai[16];
    {
      float er[8], ei[8];
      er[0] = 1.f; ei[0] = 0.f; E(1.f, er[1], ei[1]);
#pragma unroll
      for (int j = 2; j < 8; ++j) cmul(er[j / 2], ei[j / 2], er[j - j / 2], ei[j - j / 2], er[j], ei[j]);
      ar[0] = wr; ai[0] = wi;
#pragma unroll
      for (int j = 1; j < 8; ++j) cmul(wr, wi, er[j], ei[j], ar[j], ai[j]);
#pragma unroll
      for (int j = 0; j < 8; ++j) cmul(ar[j], ai[j], e128r, e128i, ar[8 + j], ai[8 + j]);
#pragma unroll
      for (int j = 0; j < 16; ++j) stP(j, ar[j], ai[j]);
    }
    float br[8], bi[8];
#pragma unroll
    for (int j = 0; j < 8; ++j) { cmul(ar[2 * (j & 3) + 8 * (j >> 2)], ai[2 * (j & 3) + 8 * (j >> 2)], e256r, e256i, br[j], bi[j]); stP(16 + j, br[j], bi[j]); }
    float cr[8], ci[8];
#pragma unroll
    for (int j = 0; j < 8; ++j) {
      const int e = j & 1, ip = j >> 1;
      float sr = ip < 2 ? ar[4 * e + 8 * ip] : br[2 * e + 4 * (ip - 2)], si = ip < 2 ? ai[4 * e + 8 * ip] : bi[2 * e + 4 * (ip - 2)];
      cmul(sr, si, e512r, e512i, cr[j], ci[j]); stP(24 + j, cr[j], ci[j]); }
#pragma unroll
    for (int j = 0; j < 8; ++j) {
      float sr = j < 2 ? ar[8 * j] : (j < 4 ? br[4 * (j - 2)] : cr[2 * (j - 4)]);
      float si = j < 2 ? ai[8 * j] : (j < 4 ? bi[4 * (j - 2)] : ci[2 * (j - 4)]);
      float r, i; cmul(sr, si, e1kr, e1ki, r, i); stP(32 + j, r, i); }
    { float r, i; cmul(wr, wi, e2kr, e2ki, r, i); stP(40, r, i); }
  } else {
    const int t = tid - 256, wv = w - 4;
    const unsigned scr = (unsigned)(size_t)sm + PP_S + 64;
    if (wv == 3 && pb < CTB) {
      unsigned long long pc; unsigned o1, o2;
      asm volatile("s_getpc_b64 %0\n.Lct%=:\n\ts_mov_b32 %1, .Lct%=-_Z2k12P1\n\ts_mov_b32 %2, _Z2k22P2-_Z2k12P1" : "=s"(pc), "=s"(o1), "=s"(o2));
#pragma unroll
      for (int i = 0; i < 4; ++i) {
        const unsigned o = (unsigned)(i * 64 + l) * 128u;
        if (o < o2 + CTK2) dma4v((const char*)(pc - o1) + o, scr);
      }
    }
    const u16* tw = ((l & 31) < 14 ? p.wg : p.wu) + (size_t)(lt * F_ + 896 * (pb & 1) + ((l & 31) < 14 ? (l & 31) : (l & 31) - 14) * 64);
    const u16* tw2 = ((l & 31) < 14 ? p.wg : p.wu) + (size_t)((8 * (pb >> 3) + 2 * wv + (l >> 5)) * F_ + 896 * (pb & 1) + ((l & 31) < 14 ? (l & 31) : (l & 31) - 14) * 64);
    if (pb < 32 && (l & 31) < 28) {
#pragma unroll
      for (int s = TS0; s < TS0 + TF0; ++s) dma4v(tw2 + (size_t)(32 * s) * F_, scr);
    }
    if (wv < 2) {}
    else if (wv == 2) dma4(2u * ((512 * ((pb & 7) >> 1) + 16 * lt + (l >> 3)) * D_) + (l & 7) * 128, (const char*)p.u, scr);
    else dma4(2u * ((512 * ((pb & 7) >> 1) + 16 * lt + 8 + (l >> 3)) * D_) + (l & 7) * 128, (const char*)p.u, scr);
    const u32x4 ut0 = *(const u32x4*)(p.u + (size_t)(64 * lt + (t >> 3)) * D_ + 64 * dtile + 8 * (t & 7));
    const u32x4 ut1 = *(const u32x4*)(p.u + (size_t)(64 * lt + 32 + (t >> 3)) * D_ + 64 * dtile + 8 * (t & 7));
#pragma unroll
    for (int k = 0; k < 2; ++k) {
      const int i = wv + 4 * k, f = 7 * pb + i;
      if (i < 7) glds16(p.wd + (size_t)(16 * (f >> 4) + (l >> 2)) * D_ + 32 * (f & 15) + 8 * (l & 3), sm + PP_W + i * 1024);
    }
    const u32x4 gwv = *(const u32x4*)(p.gw + 8 * l);
    const u32x4 gx0 = *(const u32x4*)(p.u + (size_t)(8 * pb + 2 * wv) * D_ + 8 * l);
    const u32x4 gx1 = *(const u32x4*)(p.u + (size_t)(8 * pb + 2 * wv + 1) * D_ + 8 * l);
    {
      const u32x4 ones = {~0u, ~0u, ~0u, ~0u};
      st16wt(p.ws, (unsigned)WS_AT + (unsigned)(pb * 8192 + t * 16), ones);
      st16wt(p.ws, (unsigned)WS_AT + (unsigned)(pb * 8192 + 4096 + t * 16), ones);
    }
    float s0 = 0.f, s1 = 0.f;
#pragma unroll
    for (int e = 0; e < 4; ++e) {
      const float gl = bflo(gwv[e]), gh = bfhi(gwv[e]);
      unsigned p0 = pk_bf16(bflo(gx0[e]) * gl, bfhi(gx0[e]) * gh), p1 = pk_bf16(bflo(gx1[e]) * gl, bfhi(gx1[e]) * gh);
      s0 += bflo(p0); s0 += bfhi(p0); s1 += bflo(p1); s1 += bfhi(p1);
    }
    s0 = wsum63(s0); s1 = wsum63(s1);
    if (l == 63) {
      f32x2 gg = {bfr(sigmoidf_(bfr(s0))), bfr(sigmoidf_(bfr(s1)))};
      *(f32x2*)(p.ws + WS_G + (8 * pb + 2 * wv) * 4) = gg;
    }
#pragma unroll
    for (int e = 0; e < 4; ++e) {
      *(LDS unsigned*)(sm + PP_T + (t >> 3) * 132 + (t & 7) * 16 + 4 * e) = ut0[e];
      *(LDS unsigned*)(sm + PP_T + (32 + (t >> 3)) * 132 + (t & 7) * 16 + 4 * e) = ut1[e];
    }
    if (pb < 32) {
      for (int s = TS0 + TF0; s < 16; ++s) {
        if ((l & 31) < 28) dma4v(tw2 + (size_t)(32 * s) * F_, scr);
        __builtin_amdgcn_s_sleep(TFSLP);
      }
      asm volatile("s_waitcnt vmcnt(%0)" :: "n"(16 - TS0 - TF0) : "memory");
    } else if (false) {
      for (int s = TS0 + 4; s < TS0 + 4 + TSPLIT; ++s) {
        if (l < 28) dma4v(tw + (size_t)(32 * s) * F_, scr);
        __builtin_amdgcn_s_sleep(TSLP);
      }
      asm volatile("s_waitcnt vmcnt(%0)" :: "n"(TSPLIT) : "memory");
    } else
    WAIT_V(0);
  }
  ST(p.ws, 224 + pb, 1);
  syncthreads();
  __builtin_amdgcn_s_setprio(PP2);
  if (w < 6) {
    const int c2 = w >= 3, T = w - 3 * c2;
    LDS char* pa = sm + PP_P + c2 * (41 * 528) + (16 * T + (l & 15)) * 528 + (l >> 4) * 16;
    LDS char* pq = sm + PP_Q + c2 * (16 * 528) + (l & 15) * 528 + (l >> 4) * 16;
    f32x4 acc0 = {0.f, 0.f, 0.f, 0.f}, acc1 = {0.f, 0.f, 0.f, 0.f};
#pragma unroll
    for (int s = 0; s < 4; ++s) {
      acc0 = __builtin_amdgcn_mfma_f32_16x16x32_f16(*(LDS f16x8*)(pa + s * 64), *(LDS f16x8*)(pq + s * 64), acc0, 0, 0, 0);
      acc1 = __builtin_amdgcn_mfma_f32_16x16x32_f16(*(LDS f16x8*)(pa + 256 + s * 64), *(LDS f16x8*)(pq + 256 + s * 64), acc1, 0, 0, 0);
    }
    const float is0 = *(LDS float*)(sm + PP_S + 8 * c2), is1 = *(LDS float*)(sm + PP_S + 8 * c2 + 4);
    LDS float* kc = (LDS float*)(sm + PP_KC) + c2 * 656;
    const int j = l & 15;
#pragma unroll
    for (int r = 0; r < 4; ++r) {
      const int row = 4 * (l >> 4) + r;
      int ci_;
      if (T == 0) ci_ = (row & 7) + 128 * (row >> 3) + 8 * j;
      else if (T == 1) ci_ = row < 8 ? 256 + (row & 3) + 64 * (row >> 2) + 4 * j : 384 + (row & 1) + 32 * ((row - 8) >> 1) + 2 * j;
      else ci_ = row < 8 ? 512 + 16 * row + j : ((row == 8 && j == 0) ? 640 : 641);
      kc[ci_] = acc0[r] * is0 + acc1[r] * is1;
    }
  }
  if (w >= 4) {
    const int t = tid - 256;
    if (w >= 6) {
      const int h = l >> 5, blk = (l >> 4) & 1, q = (l & 15) >> 2, pp = l & 3;
#pragma unroll
      for (int k = 0; k < 4; ++k) {
        const int i = 4 * (w - 6) + k, f = 7 * pb + i;
        if (i < 7) {
          LDS char* b0 = sm + PP_W + i * 1024 + (4 * h + q) * 64 + (16 * blk + 4 * pp) * 2;
          s16x4 a = trread(b0), b = trread(b0 + 8 * 64);
          s16x8 r = __builtin_shufflevector(a, b, 0, 1, 2, 3, 4, 5, 6, 7);
          st16wt(p.ws, (unsigned)WS_WD + (unsigned)(((f & 15) * KS2 + (f >> 4)) * 1024 + l * 16), __builtin_bit_cast(u32x4, r));
        }
      }
    }
#pragma unroll
    for (int k = 0; k < 2; ++k) {
      const int dd = (t >> 3) + 32 * k, lc = (t & 7) * 8;
      u32x4 o4;
#pragma unroll
      for (int e = 0; e < 4; ++e) {
        u16 a = *(LDS u16*)(sm + PP_T + (lc + 2 * e) * 132 + 2 * dd), b = *(LDS u16*)(sm + PP_T + (lc + 2 * e + 1) * 132 + 2 * dd);
        o4[e] = (unsigned)a | ((unsigned)b << 16);
      }
      st16wt(p.ws, (unsigned)WS_UT + (unsigned)(((64 * dtile + dd) * L_ + 64 * lt + lc) * 2), o4);
    }
  }
  syncthreads();
  ST(p.ws, 224 + pb, 2);
  {
    const int c2 = w >> 2, c = tid & 255, k = 255 - c, cls = w & 3;
    LDS float* kc = (LDS float*)(sm + PP_KC) + c2 * 656;
    u32x4 ov;
    if (cls < 2) {
      const float lo = kc[384 + k], hi = kc[385 + k];
      const unsigned hh = pk_bf16(hi, hi), ll = pk_bf16(lo, lo);
      ov[0] = hh; ov[1] = (k & 1) ? hh : ((hh & 0xffffu) | (ll & 0xffff0000u)); ov[2] = ll; ov[3] = ll;
    } else if (cls == 2) {
      const float x0 = kc[256 + 2 * k], x1 = kc[257 + 2 * k], x2 = kc[258 + 2 * k];
      ov[0] = pk_bf16(x2, x2); ov[1] = pk_bf16(x1, x1); ov[2] = pk_bf16(x1, x0); ov[3] = pk_bf16(x0, x0);
    } else {
      const bool c_ = l < 32;
      LDS float* b = kc + (c_ ? 128 + 4 * k : 8 * k);
      const float v0 = b[c_ ? 4 : 7], v1 = b[c_ ? 3 : 6], v2 = b[c_ ? 2 : 5], v3 = b[c_ ? 2 : 4];
      const float v4 = b[c_ ? 2 : 3], v5 = b[c_ ? 1 : 2], v6 = b[c_ ? 0 : 1], v7 = b[0];
      ov[0] = pk_bf16(v0, v1); ov[1] = pk_bf16(v2, v3); ov[2] = pk_bf16(v4, v5); ov[3] = pk_bf16(v6, v7);
    }
    st16wt(p.ws, (unsigned)WS_KR + (unsigned)(((2 * pb + c2) * KROW + 8 * c) * 2), ov);
    if (tid < 8) { u32x4 z = {0u, 0u, 0u, 0u}; st16wt(p.ws, (unsigned)WS_KR + (unsigned)(((2 * pb + (tid >> 2)) * KROW + 2048 + 8 * (tid & 3)) * 2), z); }
  }
  WAIT_V(0);
  ST(p.ws, 224 + pb, 3);
}

__global__ void __launch_bounds__(512, 4) k1(P1 p) {
  extern __shared__ __attribute__((aligned(16))) char smem[];
  LDS char* sm = (LDS char*)smem;
  if (blockIdx.x < 224) gemm1(p, sm, blockIdx.x); else prep(p, sm, blockIdx.x - 224);
}

struct P2 { const u16* dskip; u16* out; char* ws; };
constexpr int CV_K = 0;
constexpr int CV_U = 2 * 4 * 4160;
constexpr int CV_R = CV_U + 2 * 4608;
constexpr int K2_LDS = 65536 + 8192;

__device__ __forceinline__ void conv(const P2& p, LDS char* sm, int cb) {
  const int tid = threadIdx.x, w = __builtin_amdgcn_readfirstlane(tid >> 6), l = tid & 63;
  ST(p.ws, 512 + cb, 0);
  __builtin_amdgcn_s_setprio(3);
  const u16* ut = (const u16*)(p.ws + WS_UT) + (size_t)(2 * cb) * L_;
  const u16* kr = (const u16*)(p.ws + WS_KR) + (size_t)(2 * cb) * KROW;
  u32x4 uv[2];
#pragma unroll
  for (int c = 0; c < 2; ++c) uv[c] = *(const u32x4*)(ut + c * L_ + 8 * tid);
  u32x4 k0[3]; u32x2 k1v[3];
#pragma unroll
  for (int i = 0; i < 3; ++i) {
    int job = tid + 256 * i; job = job < 520 ? job : 519;
    const int c2 = job >= 260, c = job - 260 * c2;
    k0[i] = *(const u32x4*)(kr + c2 * KROW + 8 * c);
    k1v[i] = *(const u32x2*)(kr + c2 * KROW + 8 * c + 8);
    if (c == 259) { k1v[i][0] = 0u; k1v[i][1] = 0u; }
  }
  const float dsk = bf2f(p.dskip[2 * cb + (w >> 1)]);
#pragma unroll
  for (int c = 0; c < 2; ++c) {
    *(LDS u32x4*)(sm + CV_U + c * 4608 + 512 + 16 * tid) = uv[c];
    if (tid < 32) { u32x4 z = {0u, 0u, 0u, 0u}; *(LDS u32x4*)(sm + CV_U + c * 4608 + 16 * tid) = z; }
  }
#pragma unroll
  for (int i = 0; i < 3; ++i) {
    const int job = tid + 256 * i;
    if (job < 520) {
      const int c2 = job >= 260, c = job - 260 * c2;
      unsigned dd[6] = {k0[i][0], k0[i][1], k0[i][2], k0[i][3], k1v[i][0], k1v[i][1]};
      LDS char* b = sm + CV_K + c2 * (4 * 4160) + 16 * c;
      u32x4 c0 = {dd[0], dd[1], dd[2], dd[3]}, c2v = {dd[1], dd[2], dd[3], dd[4]}, c1, c3;
#pragma unroll
      for (int e = 0; e < 4; ++e) { c1[e] = __builtin_amdgcn_alignbit(dd[e + 1], dd[e], 16); c3[e] = __builtin_amdgcn_alignbit(dd[e + 2], dd[e + 1], 16); }
      *(LDS u32x4*)(b) = c0; *(LDS u32x4*)(b + 4160) = c1; *(LDS u32x4*)(b + 2 * 4160) = c2v; *(LDS u32x4*)(b + 3 * 4160) = c3;
    }
  }
  syncthreads();
  ST(p.ws, 512 + cb, 1);
  const int ch = w >> 1, rh = w & 1, j = l & 15, g = l >> 4;
  const int e = 3 - (j & 3);
  LDS char* ka = sm + CV_K + ch * (4 * 4160) + e * 4160 + 2 * (2031 - j + 8 * g - e);
  LDS char* ub = sm + CV_U + ch * 4608 + 2 * (240 + 16 * j + 8 * g);
  f32x4 acc[8];
#pragma unroll
  for (int c = 0; c < 8; ++c) acc[c] = f32x4{0.f, 0.f, 0.f, 0.f};
#pragma unroll
  for (int rr = 0; rr < 4; ++rr) {
    const int r = 4 * rh + rr;
    bf16x8 bf[8];
#pragma unroll
    for (int t = 0; t < 8; ++t) bf[t] = *(LDS bf16x8*)(ub + 512 * t - 64 * CVR);
#pragma unroll
    for (int q = 0; q < 8; ++q) {
      LDS char* a = ka - 64 * (8 * CVQ + CVR);
      s16x4 lo = *(LDS s16x4*)a, hi = *(LDS s16x4*)(a + 8);
      bf16x8 af = cat8(lo, hi);
#pragma unroll
      for (int c = q; c < 8; ++c) acc[c] = __builtin_amdgcn_mfma_f32_16x16x32_bf16(af, bf[c - q], acc[c], 0, 0, 0);
    }
  }
  ST(p.ws, 512 + cb, 2);
  LDS char* rb = sm + CV_R;
#pragma unroll
  for (int c = 0; c < 4; ++c) *(LDS f32x4*)(rb + (w * 4 + c) * 1024 + l * 16) = acc[4 * (rh ^ 1) + c];
  syncthreads();
  u16* at = (u16*)(p.ws + WS_AT) + (size_t)(2 * cb + ch) * L_;
#pragma unroll
  for (int c = 0; c < 4; ++c) {
    f32x4 o = *(LDS f32x4*)(rb + ((w ^ 1) * 4 + c) * 1024 + l * 16);
    f32x4 y = acc[4 * rh + c] + o;
    const int lpos = 256 * (4 * rh + c) + 16 * j + 4 * g;
    u32x2 uu = *(LDS u32x2*)(sm + CV_U + ch * 4608 + 512 + 2 * lpos);
    unsigned y0 = pk_bf16(y[0], y[1]), y1 = pk_bf16(y[2], y[3]);
    unsigned s0 = pk_bf16(bflo(uu[0]) * dsk, bfhi(uu[0]) * dsk), s1 = pk_bf16(bflo(uu[1]) * dsk, bfhi(uu[1]) * dsk);
    unsigned a0 = pk_bf16(bflo(y0) + bflo(s0), bfhi(y0) + bfhi(s0)), a1 = pk_bf16(bflo(y1) + bflo(s1), bfhi(y1) + bfhi(s1));
    unsigned long long v = (unsigned long long)a0 | ((unsigned long long)a1 << 32);
    if (v == ~0ull) v = 0x7FC07FC07FC07FC0ull;
    __hip_atomic_store((GLB unsigned long long*)(at + lpos), v, __ATOMIC_RELAXED, __HIP_MEMORY_SCOPE_AGENT);
  }
  ST(p.ws, 512 + cb, 3);
}

#define PFX 9
constexpr int PF = PFX;
#define APRE 4
struct F2 { bf16x8 a0, a1, b0, b1; };
#define GLD16(dst, voff, sbase, off) asm volatile("global_load_dwordx4 %0, %1, %2 offset:%3" : "=v"(dst) : "v"(voff), "s"(sbase), "i"(off))
template <int S> __device__ __forceinline__ void g2_ld(F2& f, unsigned vo, const char* A0, const char* B0) {
  const char *a = A0 + (S >> 2) * 4096, *b = B0 + (S >> 2) * 4096;
  GLD16(f.a0, vo, a, (S & 3) * 1024); GLD16(f.b0, vo, b, (S & 3) * 1024);
  GLD16(f.a1, vo, a + KS2 * 1024, (S & 3) * 1024); GLD16(f.b1, vo, b + KS2 * 1024, (S & 3) * 1024); }
template <int S> __device__ __forceinline__ void g2_pro(F2 (&f)[PF], unsigned vo, const char* A0, const char* B0) {
  g2_ld<S>(f[S], vo, A0, B0);
  if constexpr (S + 1 < PF) g2_pro<S + 1>(f, vo, A0, B0); }
template <int S> __device__ __forceinline__ void g2_step(F2 (&f)[PF], unsigned vo, const char* A0, const char* B0, f32x16 (&acc)[2][2],
                                                         unsigned va, const char* AT, unsigned long long (&av8)[4]) {
  F2& c = f[S % PF];
  constexpr int pend = (S + PF <= 28 ? PF : 28 - S) - 1;
  asm volatile("s_waitcnt vmcnt(%4)" : "+v"(c.a0), "+v"(c.a1), "+v"(c.b0), "+v"(c.b1) : "i"(4 * pend + (S + PF > 27 + APRE ? 4 : 0)) : "memory");
  acc[0][0] = MFMA32(c.a0, c.b0, acc[0][0]); acc[0][1] = MFMA32(c.a0, c.b1, acc[0][1]);
  acc[1][0] = MFMA32(c.a1, c.b0, acc[1][0]); acc[1][1] = MFMA32(c.a1, c.b1, acc[1][1]);
  SCHED();
  if constexpr (S + PF < 28) g2_ld<S + PF>(c, vo, A0, B0);
  if constexpr (S + PF == 27 + APRE) {
#pragma unroll
    for (int i = 0; i < 4; ++i)
      asm volatile("global_load_dwordx2 %0, %1, %2 sc1" : "=v"(av8[i]) : "v"(va), "s"(AT + (size_t)i * 16 * L_ * 2) : "memory");
  }
  SCHED();
  if constexpr (S + 1 < 28) g2_step<S + 1>(f, vo, A0, B0, acc, va, AT, av8);
}
__device__ __forceinline__ void gemm2(const P2& p, LDS char* sm, int b) {
  const int tid = threadIdx.x, w = __builtin_amdgcn_readfirstlane(tid >> 6), l = tid & 63;
  const int xcd = b & 7, idx = b >> 3;
  const int mt = 4 * xcd + (idx & 3), nt = idx >> 2;
#define K2NT nt
#define K2MT mt
  const char* A0 = p.ws + WS_WD + ((size_t)(2 * (K2NT)) * KS2 + 28 * w) * 1024;
  const char* B0 = p.ws + WS_H + ((size_t)(2 * (K2MT)) * KS2 + 28 * w) * 1024;
  f32x16 acc[2][2];
#pragma unroll
  for (int i = 0; i < 2; ++i)
#pragma unroll
    for (int j = 0; j < 2; ++j)
#pragma unroll
      for (int r = 0; r < 16; ++r) acc[i][j][r] = 0.f;
  ST(p.ws, 768 + b, 0);
  __builtin_amdgcn_s_setprio(G2P);
  const int tl = 16 * w + (l & 15), tok = 64 * mt + tl;
  float g;
  asm volatile("global_load_dword %0, %1, %2" : "=v"(g) : "v"(tok * 4), "s"(p.ws + WS_G));
  F2 f[PF];
  g2_pro<0>(f, l * 16, A0, B0);
  SCHED();
  unsigned long long av8[4];
  g2_step<0>(f, l * 16, A0, B0, acc, (unsigned)(((tid >> 4) * L_ + 4 * (tid & 15)) * 2),
             p.ws + WS_AT + ((size_t)(64 * nt) * L_ + 64 * mt) * 2, av8);
  asm volatile("s_waitcnt vmcnt(0)" : "+v"(g), "+v"(av8[0]), "+v"(av8[1]), "+v"(av8[2]), "+v"(av8[3]) :: "memory");
  ST(p.ws, 768 + b, 1);
  GLB unsigned long long* at = (GLB unsigned long long*)((const u16*)(p.ws + WS_AT) + (size_t)(64 * nt + (tid >> 4)) * L_ + 64 * mt + 4 * (tid & 15));
#pragma unroll
  for (int i = 0; i < 2; ++i)
#pragma unroll
    for (int j = 0; j < 2; ++j)
#pragma unroll
      for (int q = 0; q < 4; ++q) {
        f32x4 v = {acc[i][j][4 * q], acc[i][j][4 * q + 1], acc[i][j][4 * q + 2], acc[i][j][4 * q + 3]};
        *(LDS f32x4*)(sm + w * 16384 + ((2 * i + j) * 4 + q) * 1024 + l * 16) = v;
      }
  for (unsigned spins = 0; spins < (1u << 22); ++spins) {
    const bool bad = av8[0] == ~0ull || av8[1] == ~0ull || av8[2] == ~0ull || av8[3] == ~0ull;
    if (__builtin_amdgcn_ballot_w64(bad) == 0ull) break;
    __builtin_amdgcn_s_sleep(1);
#pragma unroll
    for (int i = 0; i < 4; ++i) av8[i] = __hip_atomic_load(at + (size_t)i * 16 * L_ / 4, __ATOMIC_RELAXED, __HIP_MEMORY_SCOPE_AGENT);
  }
#pragma unroll
  for (int i = 0; i < 4; ++i) *(LDS unsigned long long*)(sm + 65536 + ((tid >> 4) + 16 * i) * 128 + (tid & 15) * 8) = av8[i];
  syncthreads();
  ST(p.ws, 768 + b, 2);
  const float og = bfr(1.f - g);
  const int q = l >> 4, qq = (l & 15) >> 2, pp = l & 3;
#pragma unroll
  for (int k = 0; k < 2; ++k) {
    unsigned ov[4];
#pragma unroll
    for (int h = 0; h < 2; ++h) {
      LDS char* rp = sm + ((2 * k + (w >> 1)) * 4 + q) * 1024 + (16 * (w & 1) + (l & 15) + 32 * h) * 16;
      f32x4 s = *(LDS f32x4*)rp;
#pragma unroll
      for (int ww = 1; ww < 4; ++ww) s += *(LDS f32x4*)(rp + ww * 16384);
      s16x4 av = trread(sm + 65536 + (32 * k + 8 * q + 4 * h + qq) * 128 + (16 * w + 4 * pp) * 2);
      float o[4];
#pragma unroll
      for (int e = 0; e < 4; ++e) o[e] = bfr(g * bf2f((u16)av[e])) + bfr(og * bfr(s[e]));
      ov[2 * h] = pk_bf16(o[0], o[1]); ov[2 * h + 1] = pk_bf16(o[2], o[3]);
    }
    u32x4 o4 = {ov[0], ov[1], ov[2], ov[3]};
    *(u32x4*)(p.out + (size_t)tok * D_ + 64 * nt + 32 * k + 8 * q) = o4;
  }
  ST(p.ws, 768 + b, 3);
}

__global__ void __launch_bounds__(256, 2) k2(P2 p) {
  extern __shared__ __attribute__((aligned(16))) char smem[];
  LDS char* sm = (LDS char*)smem;
  if (blockIdx.x < 256) conv(p, sm, blockIdx.x); else gemm2(p, sm, blockIdx.x - 256);
}

#if defined(__HIP_DEVICE_COMPILE__)
#pragma clang attribute pop
#endif
extern "C" void kernel_launch(void* const* d_in, const int* in_sizes, int n_in, void* d_out, int out_size,
                              void* d_ws, size_t ws_size, hipStream_t stream) {
  P1 a;
  a.u = (const u16*)d_in[0]; a.are = (const u16*)d_in[1]; a.aim = (const u16*)d_in[2]; a.bre = (const u16*)d_in[3];
  a.bim = (const u16*)d_in[4]; a.cre = (const u16*)d_in[5]; a.cim = (const u16*)d_in[6]; a.logdt = (const u16*)d_in[7];
  a.wg = (const u16*)d_in[10]; a.wu = (const u16*)d_in[11]; a.wd = (const u16*)d_in[12];
  a.gw = (const u16*)d_in[13]; a.ws = (char*)d_ws;
  P2 b; b.dskip = (const u16*)d_in[8]; b.out = (u16*)d_out; b.ws = (char*)d_ws;
  (void)hipFuncSetAttribute((const void*)k1, hipFuncAttributeMaxDynamicSharedMemorySize, 81920);
  (void)hipFuncSetAttribute((const void*)k2, hipFuncAttributeMaxDynamicSharedMemorySize, K2_LDS);
  hipLaunchKernelGGL(k1, dim3(480), dim3(512), 81920, stream, a);
  hipLaunchKernelGGL(k2, dim3(512), dim3(256), K2_LDS, stream, b);
}
```

```cpp
#include <hip/hip_runtime.h>
#include <hip/hip_bf16.h>
#include <stdint.h>

#if defined(__HIP_DEVICE_COMPILE__)
#pragma clang attribute push(__attribute__((target("no-packed-fp32-ops"))), apply_to = function)
#endif
typedef unsigned short u16;
typedef __attribute__((ext_vector_type(4))) short s16x4;
typedef __attribute__((ext_vector_type(8))) short s16x8;
typedef __attribute__((ext_vector_type(8))) __bf16 bf16x8;
typedef __attribute__((ext_vector_type(2))) __bf16 bf16x2;
typedef __attribute__((ext_vector_type(8))) _Float16 f16x8;
typedef __attribute__((ext_vector_type(2))) _Float16 f16x2;
typedef __attribute__((ext_vector_type(16))) float f32x16;
typedef __attribute__((ext_vector_type(4))) float f32x4;
typedef __attribute__((ext_vector_type(2))) float f32x2;
typedef __attribute__((ext_vector_type(4))) unsigned u32x4;
typedef __attribute__((ext_vector_type(2))) unsigned u32x2;
#define LDS __attribute__((address_space(3)))
#define GLB __attribute__((address_space(1)))

constexpr int L_ = 2048, D_ = 512, N_ = 128, F_ = 1792;
constexpr int KS2 = F_ / 16;
constexpr int KROW = 2080;
constexpr size_t WS_H    = 0;
constexpr size_t WS_WD   = WS_H + (size_t)64 * KS2 * 1024;
constexpr size_t WS_UT   = WS_WD + (size_t)16 * KS2 * 1024;
constexpr size_t WS_KR   = WS_UT + (size_t)D_ * L_ * 2;
constexpr size_t WS_AT   = WS_KR + (size_t)D_ * KROW * 2 + 4096;
constexpr size_t WS_G    = WS_AT + (size_t)D_ * L_ * 2;
constexpr unsigned WS_BYTES = (unsigned)(WS_G + (size_t)L_ * 4);

__device__ __forceinline__ float u2f(unsigned v) { return __builtin_bit_cast(float, v); }
__device__ __forceinline__ unsigned f2u(float v) { return __builtin_bit_cast(unsigned, v); }
__device__ __forceinline__ void syncthreads() {
  __builtin_amdgcn_fence(__ATOMIC_RELEASE, "workgroup"); __builtin_amdgcn_s_barrier(); __builtin_amdgcn_fence(__ATOMIC_ACQUIRE, "workgroup"); }
__device__ __forceinline__ float bf2f(u16 v) { return u2f((unsigned)v << 16); }
__device__ __forceinline__ unsigned pk_bf16(float a, float b) {
  f32x2 t = {a, b}; bf16x2 r = __builtin_convertvector(t, bf16x2); return __builtin_bit_cast(unsigned, r); }
__device__ __forceinline__ float bflo(unsigned p) { return u2f(p << 16); }
__device__ __forceinline__ float bfhi(unsigned p) { return u2f(p & 0xffff0000u); }
__device__ __forceinline__ float bfr(float a) { return bflo(pk_bf16(a, 0.f)); }
__device__ __forceinline__ unsigned pk_f16(float a, float b) {
  f32x2 t = {a, b}; f16x2 r = __builtin_convertvector(t, f16x2); return __builtin_bit_cast(unsigned, r); }
__device__ __forceinline__ float sigmoidf_(float x) { return __builtin_amdgcn_rcpf(1.f + __builtin_amdgcn_exp2f(-1.44269504089f * x)); }
#define WAIT_V(n) asm volatile("s_waitcnt vmcnt(" #n ")" ::: "memory")
#define WAIT_L(n) asm volatile("s_waitcnt lgkmcnt(" #n ")" ::: "memory")
#define BAR() __builtin_amdgcn_s_barrier()
#define SCHED() __builtin_amdgcn_sched_barrier(0)
__device__ __forceinline__ void glds16(const void* g, LDS char* l) {
  __builtin_amdgcn_global_load_lds((const GLB void*)g, (LDS void*)l, 16, 0, 0); }

__device__ __forceinline__ void st16wt(char* ws, unsigned off, u32x4 v) {
  __builtin_amdgcn_raw_buffer_store_b128(v, __builtin_amdgcn_make_buffer_rsrc(ws, 0, WS_BYTES, 0x00020000), off, 0, 16); }
__device__ __forceinline__ s16x4 trread(LDS char* p) { return __builtin_amdgcn_ds_read_tr16_b64_v4i16((LDS s16x4*)p); }
__device__ __forceinline__ bf16x8 cat8(s16x4 a, s16x4 b) {
  s16x8 r = __builtin_shufflevector(a, b, 0, 1, 2, 3, 4, 5, 6, 7); return __builtin_bit_cast(bf16x8, r); }

struct P1 {
  const u16 *u, *are, *aim, *bre, *bim, *cre, *cim, *logdt, *wg, *wu, *wd, *gw;
  char* ws;
};

#define DSR_TR(dst, addr, off) asm volatile("ds_read_b64_tr_b16 %0, %1 offset:%2" : "=v"(dst) : "v"(addr), "i"(off))
#define DSR_128(dst, addr, off) asm volatile("ds_read_b128 %0, %1 offset:%2" : "=v"(dst) : "v"(addr), "i"(off))
__device__ __forceinline__ void dma16(unsigned voff, const char* sbase, unsigned ldsb) {
  asm volatile("s_mov_b32 m0, %2\n\ts_nop 0\n\tglobal_load_lds_dwordx4 %0, %1" :: "v"(voff), "s"(sbase), "s"(ldsb) : "memory"); }
__device__ __forceinline__ void dma4(unsigned voff, const char* sbase, unsigned ldsb) {
  asm volatile("s_mov_b32 m0, %2\n\ts_nop 0\n\tglobal_load_lds_dword %0, %1" :: "v"(voff), "s"(sbase), "s"(ldsb) : "memory"); }
struct Frag { s16x4 a0l, a0h, a1l, a1h; bf16x8 b0, b1; };
#define FRAG_WAIT(f) asm volatile("s_waitcnt lgkmcnt(0)" : "+v"(f.a0l), "+v"(f.a0h), "+v"(f.a1l), "+v"(f.a1h), "+v"(f.b0), "+v"(f.b1) :: "memory")
#define MFMA32(a, b, c) __builtin_amdgcn_mfma_f32_32x32x16_bf16(a, b, c, 0, 0, 0)
struct G1 {
  unsigned a0, a1, bx[4];
  unsigned vg, vu, vx0, vx1;
  const char *wg, *wu, *x;
  unsigned lw, lx;
};
template <int S, int KS> __device__ __forceinline__ void g1_rd0(const G1& g, Frag& f) {
  constexpr int wo = (S % 3) * 16384 + KS * 4096, xo = ((S >> 1) & 1) * 16384;
  DSR_TR(f.a0l, g.a0, wo); DSR_TR(f.a0h, g.a0, wo + 1024); DSR_128(f.b0, g.bx[2 * (S & 1) + KS], xo); }
template <int S, int KS> __device__ __forceinline__ void g1_rd1(const G1& g, Frag& f) {
  constexpr int wo = (S % 3) * 16384 + KS * 4096, xo = ((S >> 1) & 1) * 16384;
  DSR_128(f.b1, g.bx[2 * (S & 1) + KS], xo + 4096); DSR_TR(f.a1l, g.a1, wo); DSR_TR(f.a1h, g.a1, wo + 1024); }
template <int S> __device__ __forceinline__ void g1_issueW(const G1& g) {
  dma16(g.vg, g.wg + (size_t)S * 32 * F_ * 2, g.lw + (S % 3) * 16384);
  dma16(g.vu, g.wu + (size_t)S * 32 * F_ * 2, g.lw + (S % 3) * 16384 + 8192); }
template <int J> __device__ __forceinline__ void g1_issueX(const G1& g) {
  dma16(g.vx0, g.x + J * 128, g.lx + (J & 1) * 16384);
  dma16(g.vx1, g.x + J * 128, g.lx + (J & 1) * 16384 + 1024); }
template <int S> __device__ __forceinline__ void g1_stage(const G1& g, Frag& f0, Frag& f1, f32x16 (&acc)[2][2]) {
  FRAG_WAIT(f0);
  {
    const bf16x8 a0 = cat8(f0.a0l, f0.a0h), a1 = cat8(f0.a1l, f0.a1h);
    acc[0][0] = MFMA32(a0, f0.b0, acc[0][0]); SCHED();
    g1_rd0<S, 1>(g, f1); SCHED();
    acc[0][1] = MFMA32(a0, f0.b1, acc[0][1]); SCHED();
    g1_rd1<S, 1>(g, f1); SCHED();
    acc[1][0] = MFMA32(a1, f0.b0, acc[1][0]); SCHED();
    acc[1][1] = MFMA32(a1, f0.b1, acc[1][1]); SCHED();
  }
  FRAG_WAIT(f1);
  if constexpr (S < 15) {
    if constexpr (S >= 14) { WAIT_V(0); } else if constexpr ((S & 1) == 0) { WAIT_V(4); } else { WAIT_V(2); }
    BAR();
  }
  {
    const bf16x8 a0 = cat8(f1.a0l, f1.a0h), a1 = cat8(f1.a1l, f1.a1h);
    acc[0][0] = MFMA32(a0, f1.b0, acc[0][0]); SCHED();
    if constexpr (S < 15) g1_rd0<S + 1, 0>(g, f0);
    SCHED();
    acc[0][1] = MFMA32(a0, f1.b1, acc[0][1]); SCHED();
    if constexpr (S < 15) g1_rd1<S + 1, 0>(g, f0);
    SCHED();
    acc[1][0] = MFMA32(a1, f1.b0, acc[1][0]); SCHED();
    if constexpr (S + 3 < 16) g1_issueW<S + 3>(g);
    SCHED();
    acc[1][1] = MFMA32(a1, f1.b1, acc[1][1]); SCHED();
    if constexpr ((S & 1) == 1 && (S >> 1) + 2 < 8) g1_issueX<(S >> 1) + 2>(g);
    SCHED();
  }
  if constexpr (S < 15) g1_stage<S + 1>(g, f0, f1, acc);
}
__device__ __forceinline__ void gemm1(const P1& p, LDS char* sm, int b) {
  const int tid = threadIdx.x, w = __builtin_amdgcn_readfirstlane(tid >> 6), l = tid & 63;
  const int xcd = b & 7, idx = b >> 3;
  const int mt = 4 * (xcd >> 1) + (idx & 3), nt = 7 * (xcd & 1) + (idx >> 2);
  const int wm = w & 1, wn = w >> 1;
  const unsigned smb = (unsigned)(size_t)sm;
  G1 g;
  {
    const int krow = 4 * w + (l >> 4);
    g.vg = 2u * (krow * F_ + 8 * ((l & 15) ^ (2 * (l >> 4))));
    g.vu = 2u * (krow * F_ + 8 * ((l & 15) ^ (2 * ((l >> 4) | 4))));
    const int xr0 = 16 * w + (l >> 3);
    g.vx0 = 2u * (xr0 * D_ + 8 * ((l & 7) ^ ((xr0 >> 1) & 7)));
    g.vx1 = 2u * ((xr0 + 8) * D_ + 8 * ((l & 7) ^ (((xr0 + 8) >> 1) & 7)));
    g.wg = (const char*)(p.wg + nt * 128); g.wu = (const char*)(p.wu + nt * 128); g.x = (const char*)(p.u + (size_t)mt * 128 * D_);
    g.lw = smb + w * 1024; g.lx = smb + 49152 + w * 2048;
    const int h = l >> 5, blk = (l >> 4) & 1, q = (l & 15) >> 2, pp = l & 3, img = pp >> 1;
    g.a0 = smb + img * 8192 + (8 * h + q) * 256 + 32 * ((2 * wn) ^ (q | (img << 2))) + 16 * blk + 8 * (pp & 1);
    g.a1 = g.a0 ^ 32;
    const int xrow = 64 * wm + (l & 31);
    const unsigned bb = 49152 + xrow * 128 + 16 * (h ^ ((xrow >> 1) & 7));
#pragma unroll
    for (int c = 0; c < 4; ++c) g.bx[c] = smb + (bb ^ (c * 32));
  }
  f32x16 acc[2][2];
#pragma unroll
  for (int i = 0; i < 2; ++i)
#pragma unroll
    for (int j = 0; j < 2; ++j)
#pragma unroll
      for (int r = 0; r < 16; ++r) acc[i][j][r] = 0.f;
  Frag f0, f1;
  __builtin_amdgcn_s_setprio(2);
  unsigned w0, w1;
  asm volatile("global_load_dword %0, %1, %2" : "=v"(w0) : "v"(((4 * mt + 2 * wm) * KS2 + 8 * nt + 2 * wn) * 1024), "s"(p.ws + WS_H));
  asm volatile("global_load_dword %0, %1, %2" : "=v"(w1) : "v"(((4 * mt + 2 * wm + 1) * KS2 + 8 * nt + 2 * wn) * 1024), "s"(p.ws + WS_H));
  g1_issueX<0>(g); g1_issueW<0>(g); g1_issueW<1>(g); g1_issueX<1>(g); g1_issueW<2>(g);
  WAIT_V(6); BAR();
  asm volatile("" :: "v"(w0), "v"(w1));
  g1_rd0<0, 0>(g, f0); g1_rd1<0, 0>(g, f0);
  g1_stage<0>(g, f0, f1, acc);
#pragma unroll
  for (int i = 0; i < 2; ++i)
#pragma unroll
    for (int j = 0; j < 2; ++j) {
      unsigned o[4];
#pragma unroll
      for (int hh = 0; hh < 2; ++hh) {
        float hv[4];
#pragma unroll
        for (int e = 0; e < 4; e += 2) {
          unsigned gp = pk_bf16(acc[i][j][8 * hh + e], acc[i][j][8 * hh + e + 1]);
          unsigned up = pk_bf16(acc[i][j][8 * hh + 4 + e], acc[i][j][8 * hh + 4 + e + 1]);
          float g0 = bflo(gp), g1 = bfhi(gp);
          unsigned sp = pk_bf16(g0 * sigmoidf_(g0), g1 * sigmoidf_(g1));
          hv[e] = bflo(sp) * bflo(up); hv[e + 1] = bfhi(sp) * bfhi(up);
        }
        o[2 * hh] = pk_bf16(hv[0], hv[1]); o[2 * hh + 1] = pk_bf16(hv[2], hv[3]);
      }
      const int tb = 4 * mt + 2 * wm + j, ks = 8 * nt + 2 * wn + i;
      u32x4 ov = {o[0], o[1], o[2], o[3]};
      st16wt(p.ws, (unsigned)WS_H + (unsigned)((tb * KS2 + ks) * 1024 + l * 16), ov);
    }
}

constexpr int PP_P = 0;
constexpr int PP_Q = PP_P + 2 * 41 * 528;
constexpr int PP_KC = PP_Q + 2 * 16 * 528;
constexpr int PP_T = PP_KC + 2 * 656 * 4;
constexpr int PP_W = PP_T + 64 * 132;
constexpr int PP_S = PP_W + 7 * 1024;
static_assert(PP_S + 64 + 256 <= 81920, "lds");
static_assert(PP_W % 16 == 0 && PP_KC % 16 == 0 && PP_T % 16 == 0, "al");

__device__ __forceinline__ void cmul(float ar, float ai, float br, float bi, float& r, float& i) {
  r = ar * br - ai * bi; i = ar * bi + ai * br; }

template <int CTRL, int RM> __device__ __forceinline__ float dppf(float v) {
  return u2f((unsigned)__builtin_amdgcn_update_dpp(0, (int)f2u(v), CTRL, RM, 0xf, true)); }
__device__ __forceinline__ float wsum63(float v) {
  v += dppf<0x111, 0xf>(v); v += dppf<0x112, 0xf>(v); v += dppf<0x114, 0xf>(v); v += dppf<0x118, 0xf>(v);
  v += dppf<0x142, 0xa>(v); v += dppf<0x143, 0xc>(v); return v; }
__device__ __forceinline__ float wmax63(float v) {
  v = fmaxf(v, dppf<0x111, 0xf>(v)); v = fmaxf(v, dppf<0x112, 0xf>(v)); v = fmaxf(v, dppf<0x114, 0xf>(v)); v = fmaxf(v, dppf<0x118, 0xf>(v));
  v = fmaxf(v, dppf<0x142, 0xa>(v)); v = fmaxf(v, dppf<0x143, 0xc>(v)); return v; }
__device__ __forceinline__ void dma4v(const void* g, unsigned ldsb) {
  asm volatile("s_mov_b32 m0, %1\n\ts_nop 0\n\tglobal_load_lds_dword %0, off" :: "v"(g), "s"(ldsb) : "memory"); }

__device__ __forceinline__ void prep(const P1& p, LDS char* sm, int pb) {
  const int tid = threadIdx.x, w = __builtin_amdgcn_readfirstlane(tid >> 6), l = tid & 63;
  const int lt = pb >> 3, dtile = pb & 7;
  if (w < 4) {
    __builtin_amdgcn_s_setprio(3);
    const int ch = w >> 1, n = tid & 127, d = 2 * pb + ch, o = d * N_ + n;
    const float are = bf2f(p.are[o]), aim = bf2f(p.aim[o]);
    const float bre = bf2f(p.bre[o]), bim = bf2f(p.bim[o]), cre = bf2f(p.cre[o]), cim = bf2f(p.cim[o]);
    const float dt = bfr(expf(bf2f(p.logdt[d])));
    const float zr = dt * are, zi = dt * aim;
    float wr, wi;
    cmul(cre, cim, dt * bre, dt * bim, wr, wi);
    const float m = wmax63(fmaxf(fabsf(wr), fabsf(wi)));
    int ex = (int)((unsigned)__builtin_amdgcn_readlane((int)f2u(m), 63) >> 23) & 255;
    ex = ex < 2 ? 2 : (ex > 250 ? 250 : ex);
    const float sc = u2f((unsigned)(253 - ex) << 23), isc = u2f((unsigned)(ex + 1) << 23);
    wr *= sc; wi *= sc;
    if (l == 0) *(LDS float*)(sm + PP_S + 4 * w) = isc;
    const float zl = zr * 1.44269504089f, rv = zi * 0.15915494309f;
    auto E = [&](float x, float& er, float& ei) {
      float mag = __builtin_amdgcn_exp2f(zl * x), ph = __builtin_amdgcn_fractf(rv * x);
      er = mag * __builtin_amdgcn_cosf(ph); ei = mag * __builtin_amdgcn_sinf(ph); };
    LDS char* Pb = sm + PP_P + ch * (41 * 528) + n * 4;
    LDS char* Qb = sm + PP_Q + ch * (16 * 528) + n * 4;
    auto stP = [&](int row, float r, float i) { *(LDS unsigned*)(Pb + row * 528) = pk_f16(r, i); };
    auto stQ = [&](int row, float r, float i) { *(LDS unsigned*)(Qb + row * 528) = pk_f16(r, -i); };
    {
      float qr[16], qi[16];
      qr[0] = 1.f; qi[0] = 0.f; E(8.f, qr[1], qi[1]);
#pragma unroll
      for (int j = 2; j < 16; ++j) cmul(qr[j / 2], qi[j / 2], qr[j - j / 2], qi[j - j / 2], qr[j], qi[j]);
#pragma unroll
      for (int j = 0; j < 16; ++j) stQ(j, qr[j], qi[j]);
    }
    float e128r, e128i, e256r, e256i, e512r, e512i, e1kr, e1ki, e2kr, e2ki;
    E(128.f, e128r, e128i); E(256.f, e256r, e256i); E(512.f, e512r, e512i); E(1024.f, e1kr, e1ki); E(2048.f, e2kr, e2ki);
    float ar[16], ai[16];
    {
      float er[8], ei[8];
      er[0] = 1.f; ei[0] = 0.f; E(1.f, er[1], ei[1]);
#pragma unroll
      for (int j = 2; j < 8; ++j) cmul(er[j / 2], ei[j / 2], er[j - j / 2], ei[j - j / 2], er[j], ei[j]);
      ar[0] = wr; ai[0] = wi;
#pragma unroll
      for (int j = 1; j < 8; ++j) cmul(wr, wi, er[j], ei[j], ar[j], ai[j]);
#pragma unroll
      for (int j = 0; j < 8; ++j) cmul(ar[j], ai[j], e128r, e128i, ar[8 + j], ai[8 + j]);
#pragma unroll
      for (int j = 0; j < 16; ++j) stP(j, ar[j], ai[j]);
    }
    float br[8], bi[8];
#pragma unroll
    for (int j = 0; j < 8; ++j) { cmul(ar[2 * (j & 3) + 8 * (j >> 2)], ai[2 * (j & 3) + 8 * (j >> 2)], e256r, e256i, br[j], bi[j]); stP(16 + j, br[j], bi[j]); }
    float cr[8], ci[8];
#pragma unroll
    for (int j = 0; j < 8; ++j) {
      const int e = j & 1, ip = j >> 1;
      float sr = ip < 2 ? ar[4 * e + 8 * ip] : br[2 * e + 4 * (ip - 2)], si = ip < 2 ? ai[4 * e + 8 * ip] : bi[2 * e + 4 * (ip - 2)];
      cmul(sr, si, e512r, e512i, cr[j], ci[j]); stP(24 + j, cr[j], ci[j]); }
#pragma unroll
    for (int j = 0; j < 8; ++j) {
      float sr = j < 2 ? ar[8 * j] : (j < 4 ? br[4 * (j - 2)] : cr[2 * (j - 4)]);
      float si = j < 2 ? ai[8 * j] : (j < 4 ? bi[4 * (j - 2)] : ci[2 * (j - 4)]);
      float r, i; cmul(sr, si, e1kr, e1ki, r, i); stP(32 + j, r, i); }
    { float r, i; cmul(wr, wi, e2kr, e2ki, r, i); stP(40, r, i); }
  } else {
    const int t = tid - 256, wv = w - 4;
    const unsigned scr = (unsigned)(size_t)sm + PP_S + 64;
    if (wv == 3) {
      unsigned long long pc; unsigned o1, o2;
      asm volatile("s_getpc_b64 %0\n.Lct%=:\n\ts_mov_b32 %1, .Lct%=-_Z2k12P1\n\ts_mov_b32 %2, _Z2k22P2-_Z2k12P1" : "=s"(pc), "=s"(o1), "=s"(o2));
#pragma unroll
      for (int i = 0; i < 4; ++i) {
        const unsigned o = (unsigned)(i * 64 + l) * 128u;
        if (o2 <= 24576u && o < o2 + 8192u) dma4v((const char*)(pc - o1) + o, scr);
      }
    }
    if (pb < 32 && (l & 31) < 28) {
      const u16* tw = ((l & 31) < 14 ? p.wg : p.wu) +
          (size_t)((8 * (pb >> 3) + 2 * wv + (l >> 5)) * F_ + 896 * (pb & 1) + ((l & 31) < 14 ? (l & 31) : (l & 31) - 14) * 64);
#pragma unroll
      for (int s = 3; s < 16; ++s) dma4v(tw + (size_t)(32 * s) * F_, scr);
    }
    if (wv >= 2) dma4(2u * ((512 * ((pb & 7) >> 1) + 16 * lt + 8 * (wv - 2) + (l >> 3)) * D_) + (l & 7) * 128, (const char*)p.u, scr);
    const u32x4 ut0 = *(const u32x4*)(p.u + (size_t)(64 * lt + (t >> 3)) * D_ + 64 * dtile + 8 * (t & 7));
    const u32x4 ut1 = *(const u32x4*)(p.u + (size_t)(64 * lt + 32 + (t >> 3)) * D_ + 64 * dtile + 8 * (t & 7));
#pragma unroll
    for (int k = 0; k < 2; ++k) {
      const int i = wv + 4 * k, f = 7 * pb + i;
      if (i < 7) glds16(p.wd + (size_t)(16 * (f >> 4) + (l >> 2)) * D_ + 32 * (f & 15) + 8 * (l & 3), sm + PP_W + i * 1024);
    }
    const u32x4 gwv = *(const u32x4*)(p.gw + 8 * l);
    const u32x4 gx0 = *(const u32x4*)(p.u + (size_t)(8 * pb + 2 * wv) * D_ + 8 * l);
    const u32x4 gx1 = *(const u32x4*)(p.u + (size_t)(8 * pb + 2 * wv + 1) * D_ + 8 * l);
    {
      const u32x4 ones = {~0u, ~0u, ~0u, ~0u};
      st16wt(p.ws, (unsigned)WS_AT + (unsigned)(pb * 8192 + t * 16), ones);
      st16wt(p.ws, (unsigned)WS_AT + (unsigned)(pb * 8192 + 4096 + t * 16), ones);
    }
    float s0 = 0.f, s1 = 0.f;
#pragma unroll
    for (int e = 0; e < 4; ++e) {
      const float gl = bflo(gwv[e]), gh = bfhi(gwv[e]);
      unsigned p0 = pk_bf16(bflo(gx0[e]) * gl, bfhi(gx0[e]) * gh), p1 = pk_bf16(bflo(gx1[e]) * gl, bfhi(gx1[e]) * gh);
      s0 += bflo(p0); s0 += bfhi(p0); s1 += bflo(p1); s1 += bfhi(p1);
    }
    s0 = wsum63(s0); s1 = wsum63(s1);
    if (l == 63) {
      f32x2 gg = {bfr(sigmoidf_(bfr(s0))), bfr(sigmoidf_(bfr(s1)))};
      *(f32x2*)(p.ws + WS_G + (8 * pb + 2 * wv) * 4) = gg;
    }
#pragma unroll
    for (int e = 0; e < 4; ++e) {
      *(LDS unsigned*)(sm + PP_T + (t >> 3) * 132 + (t & 7) * 16 + 4 * e) = ut0[e];
      *(LDS unsigned*)(sm + PP_T + (32 + (t >> 3)) * 132 + (t & 7) * 16 + 4 * e) = ut1[e];
    }
    WAIT_V(0);
  }
  syncthreads();
  __builtin_amdgcn_s_setprio(3);
  if (w < 6) {
    const int c2 = w >= 3, T = w - 3 * c2;
    LDS char* pa = sm + PP_P + c2 * (41 * 528) + (16 * T + (l & 15)) * 528 + (l >> 4) * 16;
    LDS char* pq = sm + PP_Q + c2 * (16 * 528) + (l & 15) * 528 + (l >> 4) * 16;
    f32x4 acc0 = {0.f, 0.f, 0.f, 0.f}, acc1 = {0.f, 0.f, 0.f, 0.f};
#pragma unroll
    for (int s = 0; s < 4; ++s) {
      acc0 = __builtin_amdgcn_mfma_f32_16x16x32_f16(*(LDS f16x8*)(pa + s * 64), *(LDS f16x8*)(pq + s * 64), acc0, 0, 0, 0);
      acc1 = __builtin_amdgcn_mfma_f32_16x16x32_f16(*(LDS f16x8*)(pa + 256 + s * 64), *(LDS f16x8*)(pq + 256 + s * 64), acc1, 0, 0, 0);
    }
    const float is0 = *(LDS float*)(sm + PP_S + 8 * c2), is1 = *(LDS float*)(sm + PP_S + 8 * c2 + 4);
    LDS float* kc = (LDS float*)(sm + PP_KC) + c2 * 656;
    const int j = l & 15;
#pragma unroll
    for (int r = 0; r < 4; ++r) {
      const int row = 4 * (l >> 4) + r;
      int ci_;
      if (T == 0) ci_ = (row & 7) + 128 * (row >> 3) + 8 * j;
      else if (T == 1) ci_ = row < 8 ? 256 + (row & 3) + 64 * (row >> 2) + 4 * j : 384 + (row & 1) + 32 * ((row - 8) >> 1) + 2 * j;
      else ci_ = row < 8 ? 512 + 16 * row + j : ((row == 8 && j == 0) ? 640 : 641);
      kc[ci_] = acc0[r] * is0 + acc1[r] * is1;
    }
  }
  if (w >= 4) {
    const int t = tid - 256;
    if (w >= 6) {
      const int h = l >> 5, blk = (l >> 4) & 1, q = (l & 15) >> 2, pp = l & 3;
#pragma unroll
      for (int k = 0; k < 4; ++k) {
        const int i = 4 * (w - 6) + k, f = 7 * pb + i;
        if (i < 7) {
          LDS char* b0 = sm + PP_W + i * 1024 + (4 * h + q) * 64 + (16 * blk + 4 * pp) * 2;
          s16x4 a = trread(b0), b = trread(b0 + 8 * 64);
          s16x8 r = __builtin_shufflevector(a, b, 0, 1, 2, 3, 4, 5, 6, 7);
          st16wt(p.ws, (unsigned)WS_WD + (unsigned)(((f & 15) * KS2 + (f >> 4)) * 1024 + l * 16), __builtin_bit_cast(u32x4, r));
        }
      }
    }
#pragma unroll
    for (int k = 0; k < 2; ++k) {
      const int dd = (t >> 3) + 32 * k, lc = (t & 7) * 8;
      u32x4 o4;
#pragma unroll
      for (int e = 0; e < 4; ++e) {
        u16 a = *(LDS u16*)(sm + PP_T + (lc + 2 * e) * 132 + 2 * dd), b = *(LDS u16*)(sm + PP_T + (lc + 2 * e + 1) * 132 + 2 * dd);
        o4[e] = (unsigned)a | ((unsigned)b << 16);
      }
      st16wt(p.ws, (unsigned)WS_UT + (unsigned)(((64 * dtile + dd) * L_ + 64 * lt + lc) * 2), o4);
    }
  }
  syncthreads();
  {
    const int c2 = w >> 2, c = tid & 255, k = 255 - c, cls = w & 3;
    LDS float* kc = (LDS float*)(sm + PP_KC) + c2 * 656;
    u32x4 ov;
    if (cls < 2) {
      const float lo = kc[384 + k], hi = kc[385 + k];
      const unsigned hh = pk_bf16(hi, hi), ll = pk_bf16(lo, lo);
      ov[0] = hh; ov[1] = (k & 1) ? hh : ((hh & 0xffffu) | (ll & 0xffff0000u)); ov[2] = ll; ov[3] = ll;
    } else if (cls == 2) {
      const float x0 = kc[256 + 2 * k], x1 = kc[257 + 2 * k], x2 = kc[258 + 2 * k];
      ov[0] = pk_bf16(x2, x2); ov[1] = pk_bf16(x1, x1); ov[2] = pk_bf16(x1, x0); ov[3] = pk_bf16(x0, x0);
    } else {
      const bool c_ = l < 32;
      LDS float* b = kc + (c_ ? 128 + 4 * k : 8 * k);
      const float v0 = b[c_ ? 4 : 7], v1 = b[c_ ? 3 : 6], v2 = b[c_ ? 2 : 5], v3 = b[c_ ? 2 : 4];
      const float v4 = b[c_ ? 2 : 3], v5 = b[c_ ? 1 : 2], v6 = b[c_ ? 0 : 1], v7 = b[0];
      ov[0] = pk_bf16(v0, v1); ov[1] = pk_bf16(v2, v3); ov[2] = pk_bf16(v4, v5); ov[3] = pk_bf16(v6, v7);
    }
    st16wt(p.ws, (unsigned)WS_KR + (unsigned)(((2 * pb + c2) * KROW + 8 * c) * 2), ov);
    if (tid < 8) { u32x4 z = {0u, 0u, 0u, 0u}; st16wt(p.ws, (unsigned)WS_KR + (unsigned)(((2 * pb + (tid >> 2)) * KROW + 2048 + 8 * (tid & 3)) * 2), z); }
  }
  WAIT_V(0);
}

__global__ void __launch_bounds__(512, 4) k1(P1 p) {
  extern __shared__ __attribute__((aligned(16))) char smem[];
  LDS char* sm = (LDS char*)smem;
  if (blockIdx.x < 224) gemm1(p, sm, blockIdx.x); else prep(p, sm, blockIdx.x - 224);
}

struct P2 { const u16* dskip; u16* out; char* ws; };
constexpr int CV_K = 0;
constexpr int CV_U = 2 * 4 * 4160;
constexpr int CV_R = CV_U + 2 * 4608;
constexpr int K2_LDS = 65536 + 8192;

__device__ __forceinline__ void conv(const P2& p, LDS char* sm, int cb) {
  const int tid = threadIdx.x, w = __builtin_amdgcn_readfirstlane(tid >> 6), l = tid & 63;
  __builtin_amdgcn_s_setprio(3);
  const u16* ut = (const u16*)(p.ws + WS_UT) + (size_t)(2 * cb) * L_;
  const u16* kr = (const u16*)(p.ws + WS_KR) + (size_t)(2 * cb) * KROW;
  u32x4 uv[2];
#pragma unroll
  for (int c = 0; c < 2; ++c) uv[c] = *(const u32x4*)(ut + c * L_ + 8 * tid);
  u32x4 k0[3]; u32x2 k1v[3];
#pragma unroll
  for (int i = 0; i < 3; ++i) {
    int job = tid + 256 * i; job = job < 520 ? job : 519;
    const int c2 = job >= 260, c = job - 260 * c2;
    k0[i] = *(const u32x4*)(kr + c2 * KROW + 8 * c);
    k1v[i] = *(const u32x2*)(kr + c2 * KROW + 8 * c + 8);
    if (c == 259) { k1v[i][0] = 0u; k1v[i][1] = 0u; }
  }
  const float dsk = bf2f(p.dskip[2 * cb + (w >> 1)]);
#pragma unroll
  for (int c = 0; c < 2; ++c) {
    *(LDS u32x4*)(sm + CV_U + c * 4608 + 512 + 16 * tid) = uv[c];
    if (tid < 32) { u32x4 z = {0u, 0u, 0u, 0u}; *(LDS u32x4*)(sm + CV_U + c * 4608 + 16 * tid) = z; }
  }
#pragma unroll
  for (int i = 0; i < 3; ++i) {
    const int job = tid + 256 * i;
    if (job < 520) {
      const int c2 = job >= 260, c = job - 260 * c2;
      unsigned dd[6] = {k0[i][0], k0[i][1], k0[i][2], k0[i][3], k1v[i][0], k1v[i][1]};
      LDS char* b = sm + CV_K + c2 * (4 * 4160) + 16 * c;
      u32x4 c0 = {dd[0], dd[1], dd[2], dd[3]}, c2v = {dd[1], dd[2], dd[3], dd[4]}, c1, c3;
#pragma unroll
      for (int e = 0; e < 4; ++e) { c1[e] = __builtin_amdgcn_alignbit(dd[e + 1], dd[e], 16); c3[e] = __builtin_amdgcn_alignbit(dd[e + 2], dd[e + 1], 16); }
      *(LDS u32x4*)(b) = c0; *(LDS u32x4*)(b + 4160) = c1; *(LDS u32x4*)(b + 2 * 4160) = c2v; *(LDS u32x4*)(b + 3 * 4160) = c3;
    }
  }
  syncthreads();
  __builtin_amdgcn_s_setprio(0);
  const int ch = w >> 1, rh = w & 1, j = l & 15, g = l >> 4;
  const int e = 3 - (j & 3);
  LDS char* ka = sm + CV_K + ch * (4 * 4160) + e * 4160 + 2 * (2031 - j + 8 * g - e);
  LDS char* ub = sm + CV_U + ch * 4608 + 2 * (240 + 16 * j + 8 * g);
  f32x4 acc[8];
#pragma unroll
  for (int c = 0; c < 8; ++c) acc[c] = f32x4{0.f, 0.f, 0.f, 0.f};
#pragma unroll
  for (int rr = 0; rr < 4; ++rr) {
    const int r = 4 * rh + rr;
    bf16x8 bf[8];
#pragma unroll
    for (int t = 0; t < 8; ++t) bf[t] = *(LDS bf16x8*)(ub + 512 * t - 64 * r);
#pragma unroll
    for (int q = 0; q < 8; ++q) {
      LDS char* a = ka - 64 * (8 * q + r);
      s16x4 lo = *(LDS s16x4*)a, hi = *(LDS s16x4*)(a + 8);
      bf16x8 af = cat8(lo, hi);
#pragma unroll
      for (int c = q; c < 8; ++c) acc[c] = __builtin_amdgcn_mfma_f32_16x16x32_bf16(af, bf[c - q], acc[c], 0, 0, 0);
    }
  }
  LDS char* rb = sm + CV_R;
#pragma unroll
  for (int c = 0; c < 4; ++c) *(LDS f32x4*)(rb + (w * 4 + c) * 1024 + l * 16) = acc[4 * (rh ^ 1) + c];
  syncthreads();
  u16* at = (u16*)(p.ws + WS_AT) + (size_t)(2 * cb + ch) * L_;
#pragma unroll
  for (int c = 0; c < 4; ++c) {
    f32x4 o = *(LDS f32x4*)(rb + ((w ^ 1) * 4 + c) * 1024 + l * 16);
    f32x4 y = acc[4 * rh + c] + o;
    const int lpos = 256 * (4 * rh + c) + 16 * j + 4 * g;
    u32x2 uu = *(LDS u32x2*)(sm + CV_U + ch * 4608 + 512 + 2 * lpos);
    unsigned y0 = pk_bf16(y[0], y[1]), y1 = pk_bf16(y[2], y[3]);
    unsigned s0 = pk_bf16(bflo(uu[0]) * dsk, bfhi(uu[0]) * dsk), s1 = pk_bf16(bflo(uu[1]) * dsk, bfhi(uu[1]) * dsk);
    unsigned a0 = pk_bf16(bflo(y0) + bflo(s0), bfhi(y0) + bfhi(s0)), a1 = pk_bf16(bflo(y1) + bflo(s1), bfhi(y1) + bfhi(s1));
    unsigned long long v = (unsigned long long)a0 | ((unsigned long long)a1 << 32);
    if (v == ~0ull) v = 0x7FC07FC07FC07FC0ull;
    __hip_atomic_store((GLB unsigned long long*)(at + lpos), v, __ATOMIC_RELAXED, __HIP_MEMORY_SCOPE_AGENT);
  }
}

constexpr int PF = 9;
struct F2 { bf16x8 a0, a1, b0, b1; };
#define GLD16(dst, voff, sbase, off) asm volatile("global_load_dwordx4 %0, %1, %2 offset:%3" : "=v"(dst) : "v"(voff), "s"(sbase), "i"(off))
template <int S> __device__ __forceinline__ void g2_ld(F2& f, unsigned vo, const char* A0, const char* B0) {
  const char *a = A0 + (S >> 2) * 4096, *b = B0 + (S >> 2) * 4096;
  GLD16(f.a0, vo, a, (S & 3) * 1024); GLD16(f.b0, vo, b, (S & 3) * 1024);
  GLD16(f.a1, vo, a + KS2 * 1024, (S & 3) * 1024); GLD16(f.b1, vo, b + KS2 * 1024, (S & 3) * 1024); }
template <int S> __device__ __forceinline__ void g2_pro(F2 (&f)[PF], unsigned vo, const char* A0, const char* B0) {
  g2_ld<S>(f[S], vo, A0, B0);
  if constexpr (S + 1 < PF) g2_pro<S + 1>(f, vo, A0, B0); }
template <int S> __device__ __forceinline__ void g2_step(F2 (&f)[PF], unsigned vo, const char* A0, const char* B0, f32x16 (&acc)[2][2],
                                                         unsigned va, const char* AT, unsigned long long (&av8)[4]) {
  F2& c = f[S % PF];
  constexpr int pend = (S + PF <= 28 ? PF : 28 - S) - 1;
  asm volatile("s_waitcnt vmcnt(%4)" : "+v"(c.a0), "+v"(c.a1), "+v"(c.b0), "+v"(c.b1) : "i"(4 * pend + (S + PF > 27 + 4 ? 4 : 0)) : "memory");
  acc[0][0] = MFMA32(c.a0, c.b0, acc[0][0]); acc[0][1] = MFMA32(c.a0, c.b1, acc[0][1]);
  acc[1][0] = MFMA32(c.a1, c.b0, acc[1][0]); acc[1][1] = MFMA32(c.a1, c.b1, acc[1][1]);
  SCHED();
  if constexpr (S + PF < 28) g2_ld<S + PF>(c, vo, A0, B0);
  if constexpr (S + PF == 27 + 4) {
#pragma unroll
    for (int i = 0; i < 4; ++i)
      asm volatile("global_load_dwordx2 %0, %1, %2 sc1" : "=v"(av8[i]) : "v"(va), "s"(AT + (size_t)i * 16 * L_ * 2) : "memory");
  }
  SCHED();
  if constexpr (S + 1 < 28) g2_step<S + 1>(f, vo, A0, B0, acc, va, AT, av8);
}
__device__ __forceinline__ void gemm2(const P2& p, LDS char* sm, int b) {
  const int tid = threadIdx.x, w = __builtin_amdgcn_readfirstlane(tid >> 6), l = tid & 63;
  const int xcd = b & 7, idx = b >> 3;
  const int mt = 4 * xcd + (idx & 3), nt = idx >> 2;
  const char* A0 = p.ws + WS_WD + ((size_t)(2 * nt) * KS2 + 28 * w) * 1024;
  const char* B0 = p.ws + WS_H + ((size_t)(2 * mt) * KS2 + 28 * w) * 1024;
  f32x16 acc[2][2];
#pragma unroll
  for (int i = 0; i < 2; ++i)
#pragma unroll
    for (int j = 0; j < 2; ++j)
#pragma unroll
      for (int r = 0; r < 16; ++r) acc[i][j][r] = 0.f;
  __builtin_amdgcn_s_setprio(0);
  const int tl = 16 * w + (l & 15), tok = 64 * mt + tl;
  float g;
  asm volatile("global_load_dword %0, %1, %2" : "=v"(g) : "v"(tok * 4), "s"(p.ws + WS_G));
  unsigned w0;
  F2 f[PF];
  g2_pro<0>(f, l * 16, A0, B0);
  asm volatile("global_load_dword %0, %1, %2" : "=v"(w0) : "v"(((64 * mt + 16 * w) * D_ + 64 * nt) * 2), "s"(p.out));
  SCHED();
  unsigned long long av8[4];
  g2_step<0>(f, l * 16, A0, B0, acc, (unsigned)(((tid >> 4) * L_ + 4 * (tid & 15)) * 2),
             p.ws + WS_AT + ((size_t)(64 * nt) * L_ + 64 * mt) * 2, av8);
  asm volatile("s_waitcnt vmcnt(0)" : "+v"(g), "+v"(av8[0]), "+v"(av8[1]), "+v"(av8[2]), "+v"(av8[3]), "+v"(w0) :: "memory");
  GLB unsigned long long* at = (GLB unsigned long long*)((const u16*)(p.ws + WS_AT) + (size_t)(64 * nt + (tid >> 4)) * L_ + 64 * mt + 4 * (tid & 15));
#pragma unroll
  for (int i = 0; i < 2; ++i)
#pragma unroll
    for (int j = 0; j < 2; ++j)
#pragma unroll
      for (int q = 0; q < 4; ++q) {
        f32x4 v = {acc[i][j][4 * q], acc[i][j][4 * q + 1], acc[i][j][4 * q + 2], acc[i][j][4 * q + 3]};
        *(LDS f32x4*)(sm + w * 16384 + ((2 * i + j) * 4 + q) * 1024 + l * 16) = v;
      }
  for (unsigned spins = 0; spins < (1u << 22); ++spins) {
    const bool bad = av8[0] == ~0ull || av8[1] == ~0ull || av8[2] == ~0ull || av8[3] == ~0ull;
    if (__builtin_amdgcn_ballot_w64(bad) == 0ull) break;
    __builtin_amdgcn_s_sleep(1);
#pragma unroll
    for (int i = 0; i < 4; ++i) av8[i] = __hip_atomic_load(at + (size_t)i * 16 * L_ / 4, __ATOMIC_RELAXED, __HIP_MEMORY_SCOPE_AGENT);
  }
#pragma unroll
  for (int i = 0; i < 4; ++i) *(LDS unsigned long long*)(sm + 65536 + ((tid >> 4) + 16 * i) * 128 + (tid & 15) * 8) = av8[i];
  syncthreads();
  const float og = bfr(1.f - g);
  const int q = l >> 4, qq = (l & 15) >> 2, pp = l & 3;
#pragma unroll
  for (int k = 0; k < 2; ++k) {
    unsigned ov[4];
#pragma unroll
    for (int h = 0; h < 2; ++h) {
      LDS char* rp = sm + ((2 * k + (w >> 1)) * 4 + q) * 1024 + (16 * (w & 1) + (l & 15) + 32 * h) * 16;
      f32x4 s = *(LDS f32x4*)rp;
#pragma unroll
      for (int ww = 1; ww < 4; ++ww) s += *(LDS f32x4*)(rp + ww * 16384);
      s16x4 av = trread(sm + 65536 + (32 * k + 8 * q + 4 * h + qq) * 128 + (16 * w + 4 * pp) * 2);
      float o[4];
#pragma unroll
      for (int e = 0; e < 4; ++e) o[e] = bfr(g * bf2f((u16)av[e])) + bfr(og * bfr(s[e]));
      ov[2 * h] = pk_bf16(o[0], o[1]); ov[2 * h + 1] = pk_bf16(o[2], o[3]);
    }
    u32x4 o4 = {ov[0], ov[1], ov[2], ov[3]};
    *(u32x4*)(p.out + (size_t)tok * D_ + 64 * nt + 32 * k + 8 * q) = o4;
  }
}

__global__ void __launch_bounds__(256, 2) k2(P2 p) {
  extern __shared__ __attribute__((aligned(16))) char smem[];
  LDS char* sm = (LDS char*)smem;
  if (blockIdx.x < 256) conv(p, sm, blockIdx.x); else gemm2(p, sm, blockIdx.x - 256);
}

#if defined(__HIP_DEVICE_COMPILE__)
#pragma clang attribute pop
#endif
extern "C" void kernel_launch(void* const* d_in, const int* in_sizes, int n_in, void* d_out, int out_size,
                              void* d_ws, size_t ws_size, hipStream_t stream) {
  P1 a;
  a.u = (const u16*)d_in[0]; a.are = (const u16*)d_in[1]; a.aim = (const u16*)d_in[2]; a.bre = (const u16*)d_in[3];
  a.bim = (const u16*)d_in[4]; a.cre = (const u16*)d_in[5]; a.cim = (const u16*)d_in[6]; a.logdt = (const u16*)d_in[7];
  a.wg = (const u16*)d_in[10]; a.wu = (const u16*)d_in[11]; a.wd = (const u16*)d_in[12];
  a.gw = (const u16*)d_in[13]; a.ws = (char*)d_ws;
  P2 b; b.dskip = (const u16*)d_in[8]; b.out = (u16*)d_out; b.ws = (char*)d_ws;
  (void)hipFuncSetAttribute((const void*)k1, hipFuncAttributeMaxDynamicSharedMemorySize, 81920);
  (void)hipFuncSetAttribute((const void*)k2, hipFuncAttributeMaxDynamicSharedMemorySize, K2_LDS);
  hipLaunchKernelGGL(k1, dim3(480), dim3(512), 81920, stream, a);
  hipLaunchKernelGGL(k2, dim3(512), dim3(256), K2_LDS, stream, b);
}
```
